```python
import torch

def _softmax(x, axis=-1):
    return torch.softmax(x, dim=axis)
def _gelu(x):
    return torch.nn.functional.gelu(x, approximate="tanh")
def _silu(x):
    return torch.nn.functional.silu(x)
def _sigmoid(x):
    return torch.sigmoid(x)
def _layernorm(x, axis=-1, eps=1e-5):
    m = x.mean(dim=axis, keepdim=True)
    v = x.var(dim=axis, keepdim=True, unbiased=False)
    return (x - m) / torch.sqrt(v + eps)
def _scatter_add(tgt, idx, src, axis=0):
    return tgt.clone().index_add_(axis, idx, src)
def _segment_sum(src, seg, n):
    out = torch.zeros((n,) + src.shape[1:], dtype=src.dtype)
    return out.index_add_(0, seg, src)
def _pad(x, pw):
    flat = tuple(v for pair in reversed(pw) for v in pair)
    return torch.nn.functional.pad(x, flat)

kernel_name = 'concat_heads__sliding_local_dilated__softmax_full_mha__prompt'

def setup_inputs():

    _w = (lambda a: (lambda t: t.to(torch.bfloat16) if t.is_floating_point() else t)(torch.as_tensor(a)))
    def _A_setup():
        B, S, D = 1, 2048, 512
        return {
            "x": _w((lambda *s: torch.randn(*s, dtype=torch.bfloat16) * 0.4)(B, S, D)),
            "wqkv": _w((lambda *s: torch.randn(*s, dtype=torch.bfloat16) * 0.4)(D, 3 * D)),
            "wo": _w((lambda *s: torch.randn(*s, dtype=torch.bfloat16) * 0.05)(D, D)),
        }
    _A_in = _A_setup()

    def _B_setup():
        B, S, D = 1, 2048, 512
        return {
            "x": _w((lambda *s: torch.randn(*s, dtype=torch.bfloat16) * 0.2)(B, S, D)),
            "wqkv": _w((lambda *s: torch.randn(*s, dtype=torch.bfloat16) * 0.4)(D, 3 * D)),
            "wo": _w((lambda *s: torch.randn(*s, dtype=torch.bfloat16) * 0.1)(D, D)),
        }
    _B_in = _B_setup()

    return {
        "A_x": _A_in["x"],
        "A_wqkv": _A_in["wqkv"],
        "A_wo": _A_in["wo"],
        "B_x": _B_in["x"],
        "B_wqkv": _B_in["wqkv"],
        "B_wo": _B_in["wo"],
    }

def reference(A_x, A_wqkv, A_wo, B_x, B_wqkv, B_wo):
    def _A_ref(x, wqkv, wo):
        B, S, D = x.shape
        H = 32; Dh = D // H
        W, dil = 8, 2
        qkv = (lambda x, *d: x.permute(*d))((x @ wqkv).reshape(B, S, 3, H, Dh), 2, 0, 3, 1, 4)
        q, k, v = qkv[0], qkv[1], qkv[2]
        scale = Dh ** -0.5
        pos = torch.arange(S)
        off = torch.arange(W) * dil
        idx = pos[:, None] - off[None, :]
        mask = idx >= 0
        idx_c = torch.where(mask, idx, 0).reshape(-1)
        kg = k[:, :, idx_c, :].reshape(B, H, S, W, Dh)
        vg = v[:, :, idx_c, :].reshape(B, H, S, W, Dh)
        logits = torch.einsum("bhsd,bhswd->bhsw", q, kg) * scale
        logits = torch.where(mask[None, None, :, :], logits, -1e9)
        attn = _softmax(logits, axis=-1)
        o = torch.einsum("bhsw,bhswd->bhsd", attn, vg)
        return (lambda x, *d: x.permute(*d))(o, 0, 2, 1, 3).reshape(B, S, D) @ wo

    def _B_ref(x, wqkv, wo):
        B, S, D = x.shape
        H = 32; Dh = D // H
        qkv = (lambda x, *d: x.permute(*d))((x @ wqkv).reshape(B, S, 3, H, Dh), 2, 0, 3, 1, 4)
        q, k, v = qkv[0], qkv[1], qkv[2]
        scale = Dh ** -0.5
        scores = torch.einsum("bhqd,bhkd->bhqk", q, k) * scale
        p = _softmax(scores, axis=-1)
        o = torch.einsum("bhqk,bhkd->bhqd", p, v)
        return (lambda x, *d: x.permute(*d))(o, 0, 2, 1, 3).reshape(B, S, D) @ wo

    a = _A_ref(A_x, A_wqkv, A_wo)
    b = _B_ref(B_x, B_wqkv, B_wo)
    return torch.concatenate([a, b], axis=-1)
```

```cpp
#include <hip/hip_runtime.h>
#include <hip/hip_bf16.h>
#include <stdint.h>

typedef __attribute__((ext_vector_type(8))) __bf16 bf16x8;
typedef __attribute__((ext_vector_type(2))) __bf16 bf16x2;
typedef __attribute__((ext_vector_type(4))) short s16x4;
typedef __attribute__((ext_vector_type(16))) float f32x16;
typedef __attribute__((ext_vector_type(4))) float f32x4;
typedef __attribute__((ext_vector_type(2))) float f32x2;
typedef __attribute__((ext_vector_type(4))) unsigned u32x4;
typedef __attribute__((ext_vector_type(2))) unsigned u32x2;
#define LAS __attribute__((address_space(3)))
typedef LAS char* lds_ptr;

constexpr int SEQ = 2048, DM = 512, NH = 32;
constexpr float QSCALE = 0.25f * 1.4426950408889634f;

constexpr size_t WS_Q = 0;
constexpr size_t WS_K = WS_Q + (2u << 20);
constexpr size_t WS_V = WS_K + (2u << 20);
constexpr size_t WS_OA = WS_V + (2u << 20);
constexpr size_t WS_OB = WS_OA + (2u << 20);
constexpr size_t WS_KN = WS_OB + (2u << 20);

constexpr int SLOT = 30720;
constexpr int SL_HALO = 16384, SL_W = 18432;
constexpr int NSLOT = 4;
constexpr int LOC_BASE = NSLOT * SLOT;
constexpr int LOC_Q = LOC_BASE, LOC_K = LOC_Q + 8192, LOC_V = LOC_K + 9216;
constexpr int LDS_BYTES = 160 * 1024;

#define MFMA32(a, b, c) __builtin_amdgcn_mfma_f32_32x32x16_bf16(a, b, c, 0, 0, 0)
#define MFMA16(a, b, c) __builtin_amdgcn_mfma_f32_16x16x32_bf16(a, b, c, 0, 0, 0)

__device__ __forceinline__ unsigned cvtpk(float lo, float hi) {
    f32x2 v = {lo, hi};
    return __builtin_bit_cast(unsigned, __builtin_convertvector(v, bf16x2));
}
__device__ __forceinline__ void glds16(const void* base, unsigned voff, unsigned lds_base) {
    unsigned sv;
    asm volatile("s_mov_b32 %0, m0\n\ts_mov_b32 m0, %3\n\ts_nop 0\n\tglobal_load_lds_dwordx4 %1, %2\n\ts_mov_b32 m0, %0"
                 : "=&s"(sv) : "v"(voff), "s"(base), "s"(lds_base) : "memory");
}
__device__ __forceinline__ bf16x8 lds_b128(lds_ptr p) { return *(const LAS bf16x8*)p; }
__device__ __forceinline__ bf16x8 lds_tr2(lds_ptr p0, lds_ptr p1) {
    s16x4 a = __builtin_amdgcn_ds_read_tr16_b64_v4i16((LAS s16x4*)p0);
    s16x4 b = __builtin_amdgcn_ds_read_tr16_b64_v4i16((LAS s16x4*)p1);
    typedef __attribute__((ext_vector_type(8))) short s16x8;
    s16x8 r = {a[0], a[1], a[2], a[3], b[0], b[1], b[2], b[3]};
    return __builtin_bit_cast(bf16x8, r);
}
__device__ __forceinline__ u32x4 pack8(const f32x16& a, int o, float s) {
    u32x4 r = {cvtpk(a[o] * s, a[o + 1] * s), cvtpk(a[o + 2] * s, a[o + 3] * s), cvtpk(a[o + 4] * s, a[o + 5] * s), cvtpk(a[o + 6] * s, a[o + 7] * s)};
    return r;
}
__device__ __forceinline__ u32x4 pack8(const f32x16& a, int o) {
    u32x4 r = {cvtpk(a[o], a[o + 1]), cvtpk(a[o + 2], a[o + 3]), cvtpk(a[o + 4], a[o + 5]), cvtpk(a[o + 6], a[o + 7])};
    return r;
}
__device__ __forceinline__ float swap32_add(float x) {
    auto r = __builtin_amdgcn_permlane32_swap(__float_as_uint(x), __float_as_uint(x), false, false);
    return __uint_as_float(r[0]) + __uint_as_float(r[1]);
}
__device__ __forceinline__ float swap32_max(float x) {
    auto r = __builtin_amdgcn_permlane32_swap(__float_as_uint(x), __float_as_uint(x), false, false);
    return fmaxf(__uint_as_float(r[0]), __uint_as_float(r[1]));
}
#define BAR() asm volatile("s_barrier" ::: "memory")
#define WAIT_LGKM0_BAR() asm volatile("s_waitcnt lgkmcnt(0)\n\ts_barrier" ::: "memory")

template <int N> __device__ __forceinline__ void wait_vm() { asm volatile("s_waitcnt vmcnt(%0)" :: "i"(N) : "memory"); }

__device__ __forceinline__ void qkv_phase(const char* xA, const char* wA, const char* xB, const char* wB, char* ws, lds_ptr lds,
                                          int pair, int t) {
    const int tid = threadIdx.x, lane = tid & 63, wave = __builtin_amdgcn_readfirstlane(tid >> 6);
    const int r = lane & 31, hh = lane >> 5;
    const unsigned lds0 = (unsigned)(uintptr_t)lds;
    const int row0 = t * 128;
    if (wave >= 4) {
        const int j = wave - 4;
        const unsigned xo0 = (unsigned)((lane >> 3) * 1024 + (((lane & 7) ^ ((lane >> 4) & 7)) << 4));
        const unsigned xo1 = (unsigned)((lane >> 3) * 1024 + (((lane & 7) ^ ((4 + (lane >> 4)) & 7)) << 4));
        unsigned wo[3];
#pragma unroll
        for (int m = 0; m < 3; ++m) { const int u = 4 * m + (lane >> 4); wo[m] = (unsigned)(((u / 3) * 4 + ((lane >> 2) & 3)) * 3072 + (u % 3) * 1024 + (lane & 3) * 16); }
        const int hp = j & 1;
        int hrow = row0 - 16 + hp * 8 + (lane >> 3); hrow = hrow < 0 ? 0 : hrow;
        const unsigned ho = (unsigned)(hrow * 1024 + (((lane & 7) ^ ((hp * 4 + (lane >> 4)) & 7)) << 4));
        auto issue = [&](int c) {
            const int kc = c & 7; const unsigned sl = lds0 + (c % NSLOT) * SLOT;
            const char* x = (c < 8 ? xB : xA) + kc * 128;
            const char* w = (c < 8 ? wB : wA) + (size_t)(kc * 64 + j * 16) * 3072 + pair * 64;
#pragma unroll
            for (int i = 0; i < 4; ++i) glds16(x + (size_t)(row0 + 32 * j + 8 * i) * 1024, (i & 1) ? xo1 : xo0, sl + (4 * j + i) * 1024);
#pragma unroll
            for (int m = 0; m < 3; ++m) glds16(w, wo[m], sl + SL_W + (3 * j + m) * 1024);
            if (c >= 8) glds16(x, ho, sl + SL_HALO + hp * 1024);
        };
#pragma unroll
        for (int c = 0; c < NSLOT; ++c) issue(c);
        f32x4 hacc = {0.f, 0.f, 0.f, 0.f};
        const int g = lane >> 4, tk = lane & 15;
        const int hx0 = SL_HALO + tk * 128 + ((g ^ ((tk >> 1) & 7)) << 4), hx1 = SL_HALO + tk * 128 + (((4 + g) ^ ((tk >> 1) & 7)) << 4);
        const int hw = SL_W + 2 * g * 768 + (1 + (j >> 1)) * 256 + ((lane >> 2) & 3) * 64 + (j & 1) * 32 + (lane & 3) * 8;
#pragma unroll
        for (int c = 0; c < 16; ++c) {
            constexpr int dummy = 0; (void)dummy;
            int hi = (c == 0) ? NSLOT - 1 : c + NSLOT - 2; if (hi > 15) hi = 15;
            int n = 0; for (int q = c + 1; q <= hi; ++q) n += (q < 8) ? 7 : 8;
            switch (n) { case 0: wait_vm<0>(); break; case 7: wait_vm<7>(); break; case 8: wait_vm<8>(); break; case 14: wait_vm<14>(); break; case 15: wait_vm<15>(); break;
                         case 16: wait_vm<16>(); break; case 21: wait_vm<21>(); break; case 22: wait_vm<22>(); break; case 23: wait_vm<23>(); break; case 24: wait_vm<24>(); break; default: wait_vm<0>(); }
            WAIT_LGKM0_BAR();
            if (c >= 1 && c - 1 + NSLOT < 16) issue(c - 1 + NSLOT);
            if (c >= 8) {
                lds_ptr s = lds + (c % NSLOT) * SLOT;
                bf16x8 x0 = lds_b128(s + hx0), x1 = lds_b128(s + hx1);
                bf16x8 w0 = lds_tr2(s + hw, s + hw + 768), w1 = lds_tr2(s + hw + 8 * 768, s + hw + 9 * 768);
                hacc = MFMA16(w0, x0, hacc); hacc = MFMA16(w1, x1, hacc);
            }
        }
        u32x2 pk = {cvtpk(hacc[0], hacc[1]), cvtpk(hacc[2], hacc[3])};
        *(LAS u32x2*)(lds + ((j >> 1) ? LOC_V : LOC_K) + (((j & 1) * 2 + (g & 1)) * 144 + tk) * 16 + (g >> 1) * 8) = pk;
    } else {
        const int row = 32 * wave + r, f = (row >> 1) & 7;
        int xa[4];
#pragma unroll
        for (int ks = 0; ks < 4; ++ks) xa[ks] = row * 128 + (((2 * ks + hh) ^ f) << 4);
        const int wa = SL_W + 2 * hh * 768 + ((lane >> 2) & 3) * 64 + ((lane >> 4) & 1) * 32 + (lane & 3) * 8;
        f32x16 a0 = {}, a1 = {}, a2 = {};
#pragma unroll
        for (int c = 0; c < 16; ++c) {
            WAIT_LGKM0_BAR();
            lds_ptr s = lds + (c % NSLOT) * SLOT;
#pragma unroll
            for (int ks = 0; ks < 4; ++ks) {
                bf16x8 x = lds_b128(s + xa[ks]);
                lds_ptr wp = s + wa + ks * 4 * 768;
                bf16x8 w0 = lds_tr2(wp, wp + 768), w1 = lds_tr2(wp + 256, wp + 256 + 768), w2 = lds_tr2(wp + 512, wp + 512 + 768);
                a0 = MFMA32(w0, x, a0); a1 = MFMA32(w1, x, a1);
                if (c < 8) a2 = MFMA32(x, w2, a2); else a2 = MFMA32(w2, x, a2);
            }
            if (c == 7) {
                const int tile = t * 4 + wave, h0 = pair * 2;
                char* q = ws + WS_Q + ((size_t)h0 * 64 + tile) * 1024 + lane * 16;
                *(u32x4*)q = pack8(a0, 0, QSCALE); *(u32x4*)(q + 65536) = pack8(a0, 8, QSCALE);
                const int rho = (r & 19) | ((r & 4) << 1) | ((r & 8) >> 1);
                char* k = ws + WS_K + ((size_t)h0 * 64 + tile) * 1024 + (hh * 32 + rho) * 16;
                *(u32x4*)k = pack8(a1, 0); *(u32x4*)(k + 65536) = pack8(a1, 8);
                char* v = ws + WS_V + ((size_t)(h0 + (r >> 4)) * 64 + tile) * 1024 + hh * 512 + (r & 15) * 16;
                *(u32x4*)v = pack8(a2, 0); *(u32x4*)(v + 256) = pack8(a2, 8);
                float n0 = 0.f, n1 = 0.f;
#pragma unroll
                for (int i = 0; i < 8; ++i) { n0 = fmaf(a1[i], a1[i], n0); n1 = fmaf(a1[8 + i], a1[8 + i], n1); }
                n0 = swap32_add(n0); n1 = swap32_add(n1);
                float n = hh ? n1 : n0;
#pragma unroll
                for (int o = 1; o < 32; o <<= 1) n = fmaxf(n, __shfl_xor(n, o));
                if (r == 0) *(float*)(ws + WS_KN + ((size_t)(h0 + hh) * 64 + tile) * 4) = n;
                a0 = f32x16{}; a1 = f32x16{}; a2 = f32x16{};
            }
        }
        *(LAS u32x4*)(lds + LOC_Q + (hh * 128 + row) * 16) = pack8(a0, 0);
        *(LAS u32x4*)(lds + LOC_Q + ((2 + hh) * 128 + row) * 16) = pack8(a0, 8);
        *(LAS u32x4*)(lds + LOC_K + (hh * 144 + 16 + row) * 16) = pack8(a1, 0);
        *(LAS u32x4*)(lds + LOC_K + ((2 + hh) * 144 + 16 + row) * 16) = pack8(a1, 8);
        *(LAS u32x4*)(lds + LOC_V + (hh * 144 + 16 + row) * 16) = pack8(a2, 0);
        *(LAS u32x4*)(lds + LOC_V + ((2 + hh) * 144 + 16 + row) * 16) = pack8(a2, 8);
    }
    WAIT_LGKM0_BAR();
}

__device__ __forceinline__ float dot2(unsigned a, unsigned b, float c) {
    return __builtin_amdgcn_fdot2_f32_bf16(__builtin_bit_cast(bf16x2, a), __builtin_bit_cast(bf16x2, b), c, false);
}
__device__ __forceinline__ float rbf(float x) { return __uint_as_float(cvtpk(x, x) & 0xffff0000u); }

__device__ __forceinline__ void local_phase(char* ws, lds_ptr lds, int pair, int t) {
    const int tid = threadIdx.x, lane = tid & 63, wave = __builtin_amdgcn_readfirstlane(tid >> 6);
    const int r = lane & 31, hh = lane >> 5, hd = wave >> 2, row = (wave & 3) * 32 + r, pos = t * 128 + row;
    const u32x4 q = *(const LAS u32x4*)(lds + LOC_Q + ((hd * 2 + hh) * 128 + row) * 16);
    lds_ptr kp = lds + LOC_K + ((hd * 2 + hh) * 144 + 16 + row) * 16;
    lds_ptr vp = lds + LOC_V + ((hd * 2 + hh) * 144 + 16 + row) * 16;
    float lg[8], m = -3.0e38f;
#pragma unroll
    for (int w = 0; w < 8; ++w) {
        const u32x4 k = *(const LAS u32x4*)(kp - 32 * w);
        float s = dot2(q[0], k[0], 0.f); s = dot2(q[1], k[1], s); s = dot2(q[2], k[2], s); s = dot2(q[3], k[3], s);
        s = rbf(swap32_add(s)) * QSCALE;
        lg[w] = (pos >= 2 * w) ? s : -1.0e9f * 1.4426950408889634f;
        m = fmaxf(m, lg[w]);
    }
    float sum = 0.f;
#pragma unroll
    for (int w = 0; w < 8; ++w) { lg[w] = __builtin_amdgcn_exp2f(lg[w] - m); sum += lg[w]; }
    const float inv = __builtin_amdgcn_rcpf(sum);
    float o[8] = {0.f, 0.f, 0.f, 0.f, 0.f, 0.f, 0.f, 0.f};
#pragma unroll
    for (int w = 0; w < 8; w += 2) {
        const unsigned p = cvtpk(lg[w] * inv, lg[w + 1] * inv);
        const u32x4 v0 = *(const LAS u32x4*)(vp - 32 * w), v1 = *(const LAS u32x4*)(vp - 32 * w - 32);
#pragma unroll
        for (int c = 0; c < 4; ++c) {
            o[2 * c] = dot2(p, __builtin_amdgcn_perm(v1[c], v0[c], 0x05040100u), o[2 * c]);
            o[2 * c + 1] = dot2(p, __builtin_amdgcn_perm(v1[c], v0[c], 0x07060302u), o[2 * c + 1]);
        }
    }
    u32x4 ov = {cvtpk(o[0], o[1]), cvtpk(o[2], o[3]), cvtpk(o[4], o[5]), cvtpk(o[6], o[7])};
    *(u32x4*)(ws + WS_OA + ((size_t)(t * 4 + (wave & 3)) * 32 + pair * 2 + hd) * 1024 + lane * 16) = ov;
}

#define SA(i) "v" #i
#define EXP1(x) "v_exp_f32 v" #x ", v" #x "\n\t"
#define STEP(c0,c1,c2,c3,c4,c5,c6,c7,c8,c9,c10,c11,c12,c13,c14,c15, CLO, NHI, NALL, KN, V0C, V1P, V0N, KOFF, VOFF0, VOFF1) \
    EXP1(c0) EXP1(c1) \
    "s_waitcnt lgkmcnt(3)\n\t" \
    "v_smfmac_f32_32x32x32_bf16 %[o], " V1P ", " NHI ", %[idx]\n\t" \
    "s_mov_b64 exec, %[msk]\n\t" \
    "ds_read_b128 " V1P ", %[av] offset:" VOFF1 "\n\t" \
    "ds_read_b128 " V0N ", %[av] offset:" VOFF0 "\n\t" \
    "s_mov_b64 exec, -1\n\t" \
    EXP1(c2) EXP1(c3) EXP1(c4) EXP1(c5) \
    "v_mfma_f32_32x32x16_bf16 " NALL ", " KN ", %[q], %[ci]\n\t" \
    "ds_read_b128 " KN ", %[ak] offset:" KOFF "\n\t" \
    EXP1(c6) EXP1(c7) EXP1(c8) EXP1(c9) \
    "s_waitcnt lgkmcnt(4)\n\t" \
    "v_smfmac_f32_32x32x32_bf16 %[o], " V0C ", " CLO ", %[idx]\n\t" \
    EXP1(c10) EXP1(c11) EXP1(c12) EXP1(c13) EXP1(c14) EXP1(c15)
#define STEP_E(KOFF, VOFF0, VOFF1) STEP(64,65,66,67,68,69,70,71,72,73,74,75,76,77,78,79, "v[64:71]", "v[88:95]", "v[80:95]", "v[100:103]", "v[104:107]", "v[116:119]", "v[108:111]", KOFF, VOFF0, VOFF1)
#define STEP_O(KOFF, VOFF0, VOFF1) STEP(80,81,82,83,84,85,86,87,88,89,90,91,92,93,94,95, "v[80:87]", "v[72:79]", "v[64:79]", "v[96:99]", "v[108:111]", "v[112:115]", "v[104:107]", KOFF, VOFF0, VOFF1)
#define MOV4(a,b,c,d,val) "v_mov_b32 v" #a ", " val "\n\tv_mov_b32 v" #b ", " val "\n\tv_mov_b32 v" #c ", " val "\n\tv_mov_b32 v" #d ", " val "\n\t"

__device__ __forceinline__ f32x16 attn_loop(bf16x8 q, f32x16 ci, unsigned ak, unsigned av) {
    f32x16 o = {};
    const unsigned idx = 0xDDDDu;
    const unsigned long long msk = 0x0000FFFF0000FFFFull;
    unsigned cnt = 8;
    asm volatile(
        MOV4(104,105,106,107, "%[one]") MOV4(108,109,110,111, "%[one]") MOV4(112,113,114,115, "%[one]") MOV4(116,117,118,119, "%[one]")
        MOV4(88,89,90,91, "0") MOV4(92,93,94,95, "0")
        "ds_read_b128 v[96:99], %[ak]\n\t"
        "ds_read_b128 v[100:103], %[ak] offset:1024\n\t"
        "s_mov_b64 exec, %[msk]\n\t"
        "ds_read_b128 v[112:115], %[av] offset:256\n\t"
        "ds_read_b128 v[104:107], %[av]\n\t"
        "s_mov_b64 exec, -1\n\t"
        "s_waitcnt lgkmcnt(3)\n\t"
        "v_mfma_f32_32x32x16_bf16 v[64:79], v[96:99], %[q], %[ci]\n\t"
        "ds_read_b128 v[96:99], %[ak] offset:2048\n\t"
        "s_nop 15\n\t"
        "1:\n\t"
        STEP_E("3072", "1024", "1280") STEP_O("4096", "2048", "2304")
        STEP_E("5120", "3072", "3328") STEP_O("6144", "4096", "4352")
        STEP_E("7168", "5120", "5376") STEP_O("8192", "6144", "6400")
        STEP_E("9216", "7168", "7424") STEP_O("10240", "8192", "8448")
        "v_add_u32 %[ak], 0x2000, %[ak]\n\t"
        "v_add_u32 %[av], 0x2000, %[av]\n\t"
        "s_sub_u32 %[cnt], %[cnt], 1\n\t"
        "s_cmp_lg_u32 %[cnt], 0\n\t"
        "s_cbranch_scc1 1b\n\t"
        "s_waitcnt lgkmcnt(0)\n\t"
        "v_smfmac_f32_32x32x32_bf16 %[o], v[116:119], v[88:95], %[idx]\n\t"
        "s_nop 15\n\t"
        : [o] "+v"(o), [ak] "+v"(ak), [av] "+v"(av), [cnt] "+s"(cnt)
        : [q] "v"(q), [ci] "v"(ci), [idx] "v"(idx), [msk] "s"(msk), [one] "s"(0x3F803F80u)
        : "memory", "scc",
          "v64","v65","v66","v67","v68","v69","v70","v71","v72","v73","v74","v75","v76","v77","v78","v79",
          "v80","v81","v82","v83","v84","v85","v86","v87","v88","v89","v90","v91","v92","v93","v94","v95",
          "v96","v97","v98","v99","v100","v101","v102","v103","v104","v105","v106","v107","v108","v109","v110","v111",
          "v112","v113","v114","v115","v116","v117","v118","v119");
    return o;
}

__device__ __forceinline__ void attn_phase(char* ws, lds_ptr lds, int h, int qb) {
    const int tid = threadIdx.x, lane = tid & 63, wave = __builtin_amdgcn_readfirstlane(tid >> 6);
    const unsigned lds0 = (unsigned)(uintptr_t)lds;
    const char* kg = ws + WS_K + (size_t)h * 65536; const char* vg = ws + WS_V + (size_t)h * 65536;
#pragma unroll
    for (int i = 0; i < 8; ++i) {
        const int tile = i * 8 + wave;
        glds16(kg + tile * 1024, lane * 16, lds0 + tile * 1024);
        glds16(vg + tile * 1024, lane * 16, lds0 + 65536 + tile * 1024);
    }
    const int qt = qb * 8 + wave;
    const u32x4 qv = *(const u32x4*)(ws + WS_Q + ((size_t)h * 64 + qt) * 1024 + lane * 16);
    float kn = *(const float*)(ws + WS_KN + ((size_t)h * 64 + lane) * 4);
#pragma unroll
    for (int o = 1; o < 64; o <<= 1) kn = fmaxf(kn, __shfl_xor(kn, o));
    float qn = dot2(qv[0], qv[0], 0.f); qn = dot2(qv[1], qv[1], qn); qn = dot2(qv[2], qv[2], qn); qn = dot2(qv[3], qv[3], qn);
    qn = swap32_add(qn);
    float mref = __builtin_sqrtf(qn * kn) * 1.0078125f;
    const bf16x8 q = __builtin_bit_cast(bf16x8, qv);
    asm volatile("s_waitcnt vmcnt(0)\n\ts_barrier" ::: "memory");
    f32x16 o;
    for (int attempt = 0; attempt < 2; ++attempt) {
        f32x16 ci;
#pragma unroll
        for (int i = 0; i < 16; ++i) ci[i] = -mref;
        o = attn_loop(q, ci, lds0 + lane * 16, lds0 + 65536 + lane * 16);
        const float l = o[8];
        if (__all(l >= 0x1p-64f && l <= 0x1p64f) || attempt == 1) break;
        float m = -3.0e38f;
        for (int kt = 0; kt < 64; ++kt) {
            const f32x16 s = MFMA32(lds_b128(lds + kt * 1024 + lane * 16), q, f32x16{});
#pragma unroll
            for (int i = 0; i < 16; ++i) m = fmaxf(m, s[i]);
        }
        mref = swap32_max(m);
    }
    const float inv = 1.0f / o[8];
    *(u32x4*)(ws + WS_OB + ((size_t)qt * 32 + h) * 1024 + lane * 16) = pack8(o, 0, inv);
}

constexpr int RED_STRIDE = 272, RED_PART = 64 * RED_STRIDE;
__device__ __forceinline__ void wo_stage(const char* wo, unsigned dst, int cs) {
    const int tid = threadIdx.x, lane = tid & 63, wave = __builtin_amdgcn_readfirstlane(tid >> 6);
    const unsigned off = (unsigned)(((lane >> 5) * 4 + ((lane >> 2) & 3)) * 1024 + ((lane >> 4) & 1) * 64 + (lane & 3) * 16);
#pragma unroll
    for (int i = 0; i < 8; ++i) { const int p = wave * 8 + i; glds16(wo + (size_t)p * 8192 + cs * 128, off, dst + p * 1024); }
}
__device__ __forceinline__ void oproj(const char* of, lds_ptr wl, lds_ptr red, char* out, int rt, int cs) {
    const int tid = threadIdx.x, lane = tid & 63, wave = __builtin_amdgcn_readfirstlane(tid >> 6);
    const int r = lane & 31, hh = lane >> 5, mi = wave & 1, kq = wave >> 1;
    const char* op = of + ((size_t)(rt * 2 + mi) * 32 + kq * 8) * 1024 + lane * 16;
    u32x4 ov[8];
#pragma unroll
    for (int i = 0; i < 8; ++i) ov[i] = *(const u32x4*)(op + i * 1024);
    lds_ptr wp = wl + (kq * 32 + hh) * 512 + ((lane >> 2) & 3) * 64 + ((lane >> 4) & 1) * 32 + (lane & 3) * 8;
    f32x16 a0 = {}, a1 = {};
#pragma unroll
    for (int i = 0; i < 8; ++i) {
        lds_ptr p = wp + i * 2048;
        const bf16x8 w0 = lds_tr2(p, p + 1024), w1 = lds_tr2(p + 256, p + 256 + 1024);
        const bf16x8 x = __builtin_bit_cast(bf16x8, ov[i]);
        a0 = MFMA32(w0, x, a0); a1 = MFMA32(w1, x, a1);
    }
    lds_ptr rp = red + kq * RED_PART + (mi * 32 + r) * RED_STRIDE + hh * 16;
#pragma unroll
    for (int g = 0; g < 4; ++g) {
        *(LAS f32x4*)(rp + g * 32) = f32x4{a0[4 * g], a0[4 * g + 1], a0[4 * g + 2], a0[4 * g + 3]};
        *(LAS f32x4*)(rp + 128 + g * 32) = f32x4{a1[4 * g], a1[4 * g + 1], a1[4 * g + 2], a1[4 * g + 3]};
    }
    WAIT_LGKM0_BAR();
    const int row = tid >> 3, c = tid & 7;
    lds_ptr sp = red + row * RED_STRIDE + c * 32;
    f32x4 s0 = *(const LAS f32x4*)sp, s1 = *(const LAS f32x4*)(sp + 16);
#pragma unroll
    for (int k = 1; k < 4; ++k) { s0 += *(const LAS f32x4*)(sp + k * RED_PART); s1 += *(const LAS f32x4*)(sp + k * RED_PART + 16); }
    u32x4 res = {cvtpk(s0[0], s0[1]), cvtpk(s0[2], s0[3]), cvtpk(s1[0], s1[1]), cvtpk(s1[2], s1[3])};
    *(u32x4*)(out + (size_t)(rt * 64 + row) * 2048 + cs * 128 + c * 16) = res;
}

__global__ __launch_bounds__(512, 2) void k_qkv(const char* xA, const char* wA, const char* xB, const char* wB, char* ws) {
    extern __shared__ __attribute__((aligned(16))) char smem[];
    const int x = blockIdx.x & 7, i = blockIdx.x >> 3;
    const int pair = (x & 1) * 8 + (i & 7), t = (x >> 1) * 4 + (i >> 3);
    qkv_phase(xA, wA, xB, wB, ws, (lds_ptr)smem, pair, t);
    local_phase(ws, (lds_ptr)smem, pair, t);
}
__global__ __launch_bounds__(512, 2) void k_attn(char* ws) {
    extern __shared__ __attribute__((aligned(16))) char smem[];
    const int x = blockIdx.x & 7, i = blockIdx.x >> 3;
    attn_phase(ws, (lds_ptr)smem, x * 4 + (i & 3), i >> 2);
}
__global__ __launch_bounds__(512, 2) void k_out(const char* woA, const char* woB, char* ws, char* out) {
    extern __shared__ __attribute__((aligned(16))) char smem[];
    lds_ptr lds = (lds_ptr)smem;
    const int x = blockIdx.x & 7, i = blockIdx.x >> 3;
    const int rt = x * 4 + (i >> 3), cs = i & 7;
    const unsigned lds0 = (unsigned)(uintptr_t)lds;
    wo_stage(woA, lds0, cs);
    asm volatile("s_waitcnt vmcnt(0)\n\ts_barrier" ::: "memory");
    oproj(ws + WS_OA, lds, lds + 65536, out, rt, cs);
    WAIT_LGKM0_BAR();
    wo_stage(woB, lds0, cs);
    asm volatile("s_waitcnt vmcnt(0)\n\ts_barrier" ::: "memory");
    oproj(ws + WS_OB, lds, lds + 65536, out + 1024, rt, cs);
}

extern "C" void kernel_launch(void* const* d_in, const int* in_sizes, int n_in, void* d_out, int out_size, void* d_ws, size_t ws_size,
                              hipStream_t stream) {
    (void)in_sizes; (void)n_in; (void)out_size; (void)ws_size;
    (void)hipFuncSetAttribute((const void*)k_qkv, hipFuncAttributeMaxDynamicSharedMemorySize, LDS_BYTES);
    (void)hipFuncSetAttribute((const void*)k_attn, hipFuncAttributeMaxDynamicSharedMemorySize, LDS_BYTES);
    (void)hipFuncSetAttribute((const void*)k_out, hipFuncAttributeMaxDynamicSharedMemorySize, LDS_BYTES);
    k_qkv<<<256, 512, LDS_BYTES, stream>>>((const char*)d_in[0], (const char*)d_in[1], (const char*)d_in[3], (const char*)d_in[4], (char*)d_ws);
    k_attn<<<256, 512, LDS_BYTES, stream>>>((char*)d_ws);
    k_out<<<256, 512, LDS_BYTES, stream>>>((const char*)d_in[2], (const char*)d_in[5], (char*)d_ws, (char*)d_out);
}
```

```cpp
#include <hip/hip_runtime.h>
#include <hip/hip_bf16.h>
#include <stdint.h>

constexpr int FS = 64;
constexpr int PSLEEP = 2, BSLEEP = 6;
constexpr int CWL = 10;

typedef __attribute__((ext_vector_type(8))) __bf16 bf16x8;
typedef __attribute__((ext_vector_type(16))) __bf16 bf16x16;
typedef __attribute__((ext_vector_type(2))) __bf16 bf16x2;
typedef __attribute__((ext_vector_type(4))) short s16x4;
typedef __attribute__((ext_vector_type(16))) float f32x16;
typedef __attribute__((ext_vector_type(8))) float f32x8;
typedef __attribute__((ext_vector_type(4))) float f32x4;
typedef __attribute__((ext_vector_type(2))) float f32x2;
typedef __attribute__((ext_vector_type(4))) unsigned u32x4;
typedef __attribute__((ext_vector_type(2))) unsigned u32x2;
typedef unsigned long long u64;
#define LAS __attribute__((address_space(3)))
#define GAS __attribute__((address_space(1)))
typedef LAS char* lds_ptr;

constexpr float QSCALE = 0.25f * 1.4426950408889634f;

constexpr unsigned WS_EPOCH = 0;
constexpr unsigned WS_FK = 4096;
constexpr unsigned WS_FQ = WS_FK + 512 * FS;
constexpr unsigned WS_FA = WS_FQ + 512 * FS;
constexpr unsigned WS_FO = WS_FA + 128 * FS;
constexpr unsigned WS_KN = 512 * 1024;
constexpr unsigned WS_Q = 1u << 20;
constexpr unsigned WS_KV = WS_Q + (2u << 20);
constexpr unsigned WS_OA = WS_KV + (4u << 20);
constexpr unsigned WS_OB = WS_OA + (2u << 20);


constexpr int SLOT = 43008, SL_HALO = 16384, SL_W = 18432;
constexpr int IMG_Q = 2 * SLOT;
constexpr int IMG_K = 0, IMG_V = 18432;
constexpr int L_WOA = 0, L_WOB0 = 65536, L_WOB1 = 131072, L_CMB = 98304;
constexpr int L_GO = L_CMB + 4 * 4608;
constexpr int L_SIG = 160 * 1024 - 16;
constexpr int LDS_BYTES = 160 * 1024;

#define MFMA32(a, b, c) __builtin_amdgcn_mfma_f32_32x32x16_bf16(a, b, c, 0, 0, 0)
#define MFMA4(a, b, c) __builtin_amdgcn_mfma_f32_4x4x4bf16_1k(a, b, c, 0, 0, 0)
#define SMFMAC(a, b, c) __builtin_amdgcn_smfmac_f32_32x32x32_bf16(a, b, c, 0xDDDD, 0, 0)
#define RLX_AGENT __ATOMIC_RELAXED, __HIP_MEMORY_SCOPE_AGENT

__device__ __forceinline__ unsigned cvtpk(float lo, float hi) {
    f32x2 v = {lo, hi};
    return __builtin_bit_cast(unsigned, __builtin_convertvector(v, bf16x2));
}
__device__ __forceinline__ void glds16(const void* base, unsigned voff, unsigned lds_base) {
    unsigned sv;
    asm volatile("s_mov_b32 %0, m0\n\ts_mov_b32 m0, %3\n\ts_nop 0\n\tglobal_load_lds_dwordx4 %1, %2\n\ts_mov_b32 m0, %0"
                 : "=&s"(sv) : "v"(voff), "s"(base), "s"(lds_base) : "memory");
}
__device__ __forceinline__ void glds16x2_sc1(const void* base, unsigned voff, unsigned lds_base) {
    asm volatile("s_mov_b32 m0, %2\n\ts_nop 0\n\tglobal_load_lds_dwordx4 %0, %1 sc1\n\tglobal_load_lds_dwordx4 %0, %1 offset:1024 sc1"
                 :: "v"(voff), "s"(base), "s"(lds_base) : "memory", "m0");
}
__device__ __forceinline__ u32x4 gld16_sc1(const void* base, unsigned voff) {
    u32x4 r; asm volatile("global_load_dwordx4 %0, %1, %2 sc1" : "=v"(r) : "v"(voff), "s"(base) : "memory"); return r;
}
__device__ __forceinline__ u64 gld8_sc1(const void* base, unsigned voff) {
    u64 r; asm volatile("global_load_dwordx2 %0, %1, %2 sc1" : "=v"(r) : "v"(voff), "s"(base) : "memory"); return r;
}
__device__ __forceinline__ void gst16_sc1(void* base, unsigned voff, u32x4 v) {
    asm volatile("global_store_dwordx4 %0, %1, %2 sc1\n\ts_nop 1" :: "v"(voff), "v"(v), "s"(base) : "memory");
}
__device__ __forceinline__ void gst4_sc1(void* base, unsigned voff, unsigned v) {
    asm volatile("global_store_dword %0, %1, %2 sc1" :: "v"(voff), "v"(v), "s"(base) : "memory");
}
__device__ __forceinline__ u32x4 pack8(const f32x16& a, int o, float s) {
    u32x4 r = {cvtpk(a[o] * s, a[o + 1] * s), cvtpk(a[o + 2] * s, a[o + 3] * s), cvtpk(a[o + 4] * s, a[o + 5] * s), cvtpk(a[o + 6] * s, a[o + 7] * s)};
    return r;
}
__device__ __forceinline__ float swap32_add(float x) {
    auto r = __builtin_amdgcn_permlane32_swap(__float_as_uint(x), __float_as_uint(x), false, false);
    return __uint_as_float(r[0]) + __uint_as_float(r[1]);
}
__device__ __forceinline__ float swap32_max(float x) {
    auto r = __builtin_amdgcn_permlane32_swap(__float_as_uint(x), __float_as_uint(x), false, false);
    return fmaxf(__uint_as_float(r[0]), __uint_as_float(r[1]));
}
template <int C> __device__ __forceinline__ float dpp_max(float x) {
    return fmaxf(x, __uint_as_float(__builtin_amdgcn_update_dpp(0u, __float_as_uint(x), C, 0xf, 0xf, true)));
}
__device__ __forceinline__ float max32(float x) {
    x = dpp_max<0x121>(x); x = dpp_max<0x122>(x); x = dpp_max<0x124>(x); x = dpp_max<0x128>(x);
    auto r = __builtin_amdgcn_permlane16_swap(__float_as_uint(x), __float_as_uint(x), false, false);
    return fmaxf(__uint_as_float(r[0]), __uint_as_float(r[1]));
}
__device__ __forceinline__ float dot2(unsigned a, unsigned b, float c) {
    return __builtin_amdgcn_fdot2_f32_bf16(__builtin_bit_cast(bf16x2, a), __builtin_bit_cast(bf16x2, b), c, false);
}
#define WAIT_LGKM0_BAR() asm volatile("s_waitcnt lgkmcnt(0)\n\ts_barrier" ::: "memory")
template <int N> __device__ __forceinline__ void wait_vm_bar() { asm volatile("s_waitcnt vmcnt(%0) lgkmcnt(0)\n\ts_barrier" :: "i"(N) : "memory"); }

template <int SL = PSLEEP> __device__ __forceinline__ void poll_flags(const char* p, unsigned voff, u64 tag) {
#pragma unroll 1
    for (unsigned spins = 0; spins < (1u << 20); ++spins) {
        u64 v;
        asm volatile("global_load_dwordx2 %0, %1, %2 sc1\n\ts_waitcnt vmcnt(0)" : "=&v"(v) : "v"(voff), "s"(p) : "memory");
        if (__all(v == tag)) break;
        __builtin_amdgcn_s_sleep(SL);
    }
}
__device__ __forceinline__ void lds_wait(lds_ptr p) {
#pragma unroll 1
    for (unsigned spins = 0; spins < (1u << 26); ++spins) {
        if (__builtin_amdgcn_readfirstlane(*(volatile LAS unsigned*)p)) break;
        __builtin_amdgcn_s_sleep(1);
    }
    asm volatile("" ::: "memory");
}
__device__ __forceinline__ void lds_wait2(lds_ptr p) {
#pragma unroll 1
    for (unsigned spins = 0; spins < (1u << 26); ++spins) {
        const u32x2 v = *(volatile LAS u32x2*)p;
        if (__builtin_amdgcn_readfirstlane(v[0] & v[1])) break;
        __builtin_amdgcn_s_sleep(1);
    }
    asm volatile("" ::: "memory");
}
__device__ __forceinline__ void put_flag(char* p, u64 tag) { __hip_atomic_store((GAS u64*)p, tag, RLX_AGENT); }

#define ATTN_BODY \
    "v_exp_f32 v64, v64\n\tv_exp_f32 v65, v65\n\tv_smfmac_f32_32x32x32_bf16 %[o3], %[v1b], v[88:95], %[idx]\n\ts_mov_b64 exec, %[msk]\n\t" \
    "ds_read_b128 %[v1b], %[ad] offset:5376\n\tds_read_b128 %[v0b], %[ad] offset:5120\n\ts_mov_b64 exec, -1\n\tv_exp_f32 v66, v66\n\t" \
    "v_exp_f32 v67, v67\n\tv_exp_f32 v68, v68\n\tv_mfma_f32_32x32x16_bf16 v[80:95], %[ka], %[q1], %[ci]\n\tv_exp_f32 v69, v69\n\t" \
    "v_exp_f32 v70, v70\n\tv_exp_f32 v71, v71\n\tv_exp_f32 v72, v72\n\tv_exp_f32 v73, v73\n\t" \
    "v_smfmac_f32_32x32x32_bf16 %[o0], %[v0a], v[64:71], %[idx]\n\tv_exp_f32 v74, v74\n\tv_exp_f32 v75, v75\n\tv_exp_f32 v76, v76\n\t" \
    "v_exp_f32 v77, v77\n\tv_exp_f32 v78, v78\n\tv_exp_f32 v79, v79\n\tv_exp_f32 v80, v80\n\t" \
    "v_exp_f32 v81, v81\n\tv_smfmac_f32_32x32x32_bf16 %[o0], %[v1a], v[72:79], %[idx]\n\tv_exp_f32 v82, v82\n\tv_exp_f32 v83, v83\n\t" \
    "v_exp_f32 v84, v84\n\tv_mfma_f32_32x32x16_bf16 v[64:79], %[ka], %[q2], %[ci]\n\tv_exp_f32 v85, v85\n\tv_exp_f32 v86, v86\n\t" \
    "v_exp_f32 v87, v87\n\tv_exp_f32 v88, v88\n\tv_exp_f32 v89, v89\n\tv_smfmac_f32_32x32x32_bf16 %[o1], %[v0a], v[80:87], %[idx]\n\t" \
    "v_exp_f32 v90, v90\n\tv_exp_f32 v91, v91\n\tv_exp_f32 v92, v92\n\tv_exp_f32 v93, v93\n\t" \
    "v_exp_f32 v94, v94\n\tv_exp_f32 v95, v95\n\tv_exp_f32 v64, v64\n\tv_exp_f32 v65, v65\n\t" \
    "v_smfmac_f32_32x32x32_bf16 %[o1], %[v1a], v[88:95], %[idx]\n\ts_waitcnt lgkmcnt(0)\n\tv_exp_f32 v66, v66\n\tv_exp_f32 v67, v67\n\t" \
    "v_exp_f32 v68, v68\n\tv_mfma_f32_32x32x16_bf16 v[80:95], %[ka], %[q3], %[ci]\n\tds_read_b128 %[ka], %[ad] offset:8192\n\tv_exp_f32 v69, v69\n\t" \
    "v_exp_f32 v70, v70\n\tv_exp_f32 v71, v71\n\tv_exp_f32 v72, v72\n\tv_exp_f32 v73, v73\n\t" \
    "v_smfmac_f32_32x32x32_bf16 %[o2], %[v0a], v[64:71], %[idx]\n\tv_exp_f32 v74, v74\n\tv_exp_f32 v75, v75\n\tv_exp_f32 v76, v76\n\t" \
    "v_exp_f32 v77, v77\n\tv_exp_f32 v78, v78\n\tv_exp_f32 v79, v79\n\tv_exp_f32 v80, v80\n\t" \
    "v_exp_f32 v81, v81\n\tv_smfmac_f32_32x32x32_bf16 %[o2], %[v1a], v[72:79], %[idx]\n\tv_exp_f32 v82, v82\n\tv_exp_f32 v83, v83\n\t" \
    "v_exp_f32 v84, v84\n\tv_mfma_f32_32x32x16_bf16 v[64:79], %[kb], %[q0], %[ci]\n\tv_exp_f32 v85, v85\n\tv_exp_f32 v86, v86\n\t" \
    "v_exp_f32 v87, v87\n\tv_exp_f32 v88, v88\n\tv_exp_f32 v89, v89\n\tv_smfmac_f32_32x32x32_bf16 %[o3], %[v0a], v[80:87], %[idx]\n\t" \
    "v_exp_f32 v90, v90\n\tv_exp_f32 v91, v91\n\tv_exp_f32 v92, v92\n\tv_exp_f32 v93, v93\n\t" \
    "v_exp_f32 v94, v94\n\tv_exp_f32 v95, v95\n\tv_exp_f32 v64, v64\n\tv_exp_f32 v65, v65\n\t" \
    "v_smfmac_f32_32x32x32_bf16 %[o3], %[v1a], v[88:95], %[idx]\n\ts_mov_b64 exec, %[msk]\n\tds_read_b128 %[v1a], %[ad] offset:9472\n\tds_read_b128 %[v0a], %[ad] offset:9216\n\t" \
    "s_mov_b64 exec, -1\n\tv_exp_f32 v66, v66\n\tv_exp_f32 v67, v67\n\tv_exp_f32 v68, v68\n\t" \
    "v_mfma_f32_32x32x16_bf16 v[80:95], %[kb], %[q1], %[ci]\n\tv_exp_f32 v69, v69\n\tv_exp_f32 v70, v70\n\tv_exp_f32 v71, v71\n\t" \
    "v_exp_f32 v72, v72\n\tv_exp_f32 v73, v73\n\tv_smfmac_f32_32x32x32_bf16 %[o0], %[v0b], v[64:71], %[idx]\n\tv_exp_f32 v74, v74\n\t" \
    "v_exp_f32 v75, v75\n\tv_exp_f32 v76, v76\n\tv_exp_f32 v77, v77\n\tv_exp_f32 v78, v78\n\t" \
    "v_exp_f32 v79, v79\n\tv_exp_f32 v80, v80\n\tv_exp_f32 v81, v81\n\tv_smfmac_f32_32x32x32_bf16 %[o0], %[v1b], v[72:79], %[idx]\n\t" \
    "v_exp_f32 v82, v82\n\tv_exp_f32 v83, v83\n\tv_exp_f32 v84, v84\n\tv_mfma_f32_32x32x16_bf16 v[64:79], %[kb], %[q2], %[ci]\n\t" \
    "v_exp_f32 v85, v85\n\tv_exp_f32 v86, v86\n\tv_exp_f32 v87, v87\n\tv_exp_f32 v88, v88\n\t" \
    "v_exp_f32 v89, v89\n\tv_smfmac_f32_32x32x32_bf16 %[o1], %[v0b], v[80:87], %[idx]\n\tv_exp_f32 v90, v90\n\tv_exp_f32 v91, v91\n\t" \
    "v_exp_f32 v92, v92\n\tv_exp_f32 v93, v93\n\tv_exp_f32 v94, v94\n\tv_exp_f32 v95, v95\n\t" \
    "v_exp_f32 v64, v64\n\tv_exp_f32 v65, v65\n\tv_smfmac_f32_32x32x32_bf16 %[o1], %[v1b], v[88:95], %[idx]\n\ts_waitcnt lgkmcnt(0)\n\t" \
    "v_exp_f32 v66, v66\n\tv_exp_f32 v67, v67\n\tv_exp_f32 v68, v68\n\tv_mfma_f32_32x32x16_bf16 v[80:95], %[kb], %[q3], %[ci]\n\t" \
    "ds_read_b128 %[kb], %[ad] offset:12288\n\tv_exp_f32 v69, v69\n\tv_exp_f32 v70, v70\n\tv_exp_f32 v71, v71\n\t" \
    "v_exp_f32 v72, v72\n\tv_exp_f32 v73, v73\n\tv_smfmac_f32_32x32x32_bf16 %[o2], %[v0b], v[64:71], %[idx]\n\tv_exp_f32 v74, v74\n\t" \
    "v_exp_f32 v75, v75\n\tv_exp_f32 v76, v76\n\tv_exp_f32 v77, v77\n\tv_exp_f32 v78, v78\n\t" \
    "v_exp_f32 v79, v79\n\tv_exp_f32 v80, v80\n\tv_exp_f32 v81, v81\n\tv_smfmac_f32_32x32x32_bf16 %[o2], %[v1b], v[72:79], %[idx]\n\t" \
    "v_exp_f32 v82, v82\n\tv_exp_f32 v83, v83\n\tv_exp_f32 v84, v84\n\tv_mfma_f32_32x32x16_bf16 v[64:79], %[ka], %[q0], %[ci]\n\t" \
    "v_exp_f32 v85, v85\n\tv_exp_f32 v86, v86\n\tv_exp_f32 v87, v87\n\tv_exp_f32 v88, v88\n\t" \
    "v_exp_f32 v89, v89\n\tv_smfmac_f32_32x32x32_bf16 %[o3], %[v0b], v[80:87], %[idx]\n\ts_barrier\n\tv_exp_f32 v90, v90\n\t" \
    "v_exp_f32 v91, v91\n\tv_exp_f32 v92, v92\n\tv_exp_f32 v93, v93\n\tv_exp_f32 v94, v94\n\t" \
    "v_exp_f32 v95, v95\n\tv_exp_f32 v64, v64\n\tv_exp_f32 v65, v65\n\tv_smfmac_f32_32x32x32_bf16 %[o3], %[v1b], v[88:95], %[idx]\n\t" \
    "s_mov_b64 exec, %[msk]\n\tds_read_b128 %[v1b], %[ad] offset:13568\n\tds_read_b128 %[v0b], %[ad] offset:13312\n\ts_mov_b64 exec, -1\n\t" \
    "v_exp_f32 v66, v66\n\tv_exp_f32 v67, v67\n\tv_exp_f32 v68, v68\n\tv_mfma_f32_32x32x16_bf16 v[80:95], %[ka], %[q1], %[ci]\n\t" \
    "v_exp_f32 v69, v69\n\tv_exp_f32 v70, v70\n\tv_exp_f32 v71, v71\n\tv_exp_f32 v72, v72\n\t" \
    "v_exp_f32 v73, v73\n\tv_smfmac_f32_32x32x32_bf16 %[o0], %[v0a], v[64:71], %[idx]\n\tv_exp_f32 v74, v74\n\tv_exp_f32 v75, v75\n\t" \
    "v_exp_f32 v76, v76\n\tv_exp_f32 v77, v77\n\tv_exp_f32 v78, v78\n\tv_exp_f32 v79, v79\n\t" \
    "v_exp_f32 v80, v80\n\tv_exp_f32 v81, v81\n\tv_smfmac_f32_32x32x32_bf16 %[o0], %[v1a], v[72:79], %[idx]\n\tv_exp_f32 v82, v82\n\t" \
    "v_exp_f32 v83, v83\n\tv_exp_f32 v84, v84\n\tv_mfma_f32_32x32x16_bf16 v[64:79], %[ka], %[q2], %[ci]\n\tv_exp_f32 v85, v85\n\t" \
    "v_exp_f32 v86, v86\n\tv_exp_f32 v87, v87\n\tv_exp_f32 v88, v88\n\tv_exp_f32 v89, v89\n\t" \
    "v_smfmac_f32_32x32x32_bf16 %[o1], %[v0a], v[80:87], %[idx]\n\tv_exp_f32 v90, v90\n\tv_exp_f32 v91, v91\n\tv_exp_f32 v92, v92\n\t" \
    "v_exp_f32 v93, v93\n\tv_exp_f32 v94, v94\n\tv_exp_f32 v95, v95\n\tv_exp_f32 v64, v64\n\t" \
    "v_exp_f32 v65, v65\n\tv_smfmac_f32_32x32x32_bf16 %[o1], %[v1a], v[88:95], %[idx]\n\ts_waitcnt lgkmcnt(0)\n\tv_exp_f32 v66, v66\n\t" \
    "v_exp_f32 v67, v67\n\tv_exp_f32 v68, v68\n\tv_mfma_f32_32x32x16_bf16 v[80:95], %[ka], %[q3], %[ci]\n\tds_read_b128 %[ka], %[ad] offset:16384\n\t" \
    "v_exp_f32 v69, v69\n\tv_exp_f32 v70, v70\n\tv_exp_f32 v71, v71\n\tv_exp_f32 v72, v72\n\t" \
    "v_exp_f32 v73, v73\n\tv_smfmac_f32_32x32x32_bf16 %[o2], %[v0a], v[64:71], %[idx]\n\tv_exp_f32 v74, v74\n\tv_exp_f32 v75, v75\n\t" \
    "v_exp_f32 v76, v76\n\tv_exp_f32 v77, v77\n\tv_exp_f32 v78, v78\n\tv_exp_f32 v79, v79\n\t" \
    "v_exp_f32 v80, v80\n\tv_exp_f32 v81, v81\n\tv_smfmac_f32_32x32x32_bf16 %[o2], %[v1a], v[72:79], %[idx]\n\tv_exp_f32 v82, v82\n\t" \
    "v_exp_f32 v83, v83\n\tv_exp_f32 v84, v84\n\tv_mfma_f32_32x32x16_bf16 v[64:79], %[kb], %[q0], %[ci]\n\tv_exp_f32 v85, v85\n\t" \
    "v_exp_f32 v86, v86\n\tv_exp_f32 v87, v87\n\tv_exp_f32 v88, v88\n\tv_exp_f32 v89, v89\n\t" \
    "v_smfmac_f32_32x32x32_bf16 %[o3], %[v0a], v[80:87], %[idx]\n\tv_exp_f32 v90, v90\n\tv_exp_f32 v91, v91\n\tv_exp_f32 v92, v92\n\t" \
    "v_exp_f32 v93, v93\n\tv_exp_f32 v94, v94\n\tv_exp_f32 v95, v95\n\tv_exp_f32 v64, v64\n\t" \
    "v_exp_f32 v65, v65\n\tv_smfmac_f32_32x32x32_bf16 %[o3], %[v1a], v[88:95], %[idx]\n\ts_mov_b64 exec, %[msk]\n\tds_read_b128 %[v1a], %[ad] offset:17664\n\t" \
    "ds_read_b128 %[v0a], %[ad] offset:17408\n\ts_mov_b64 exec, -1\n\tv_exp_f32 v66, v66\n\tv_exp_f32 v67, v67\n\t" \
    "v_exp_f32 v68, v68\n\tv_mfma_f32_32x32x16_bf16 v[80:95], %[kb], %[q1], %[ci]\n\tv_exp_f32 v69, v69\n\tv_exp_f32 v70, v70\n\t" \
    "v_exp_f32 v71, v71\n\tv_exp_f32 v72, v72\n\tv_exp_f32 v73, v73\n\tv_smfmac_f32_32x32x32_bf16 %[o0], %[v0b], v[64:71], %[idx]\n\t" \
    "v_exp_f32 v74, v74\n\tv_exp_f32 v75, v75\n\tv_exp_f32 v76, v76\n\tv_exp_f32 v77, v77\n\t" \
    "v_exp_f32 v78, v78\n\tv_exp_f32 v79, v79\n\tv_exp_f32 v80, v80\n\tv_exp_f32 v81, v81\n\t" \
    "v_smfmac_f32_32x32x32_bf16 %[o0], %[v1b], v[72:79], %[idx]\n\tv_exp_f32 v82, v82\n\tv_exp_f32 v83, v83\n\tv_exp_f32 v84, v84\n\t" \
    "v_mfma_f32_32x32x16_bf16 v[64:79], %[kb], %[q2], %[ci]\n\tv_exp_f32 v85, v85\n\tv_exp_f32 v86, v86\n\tv_exp_f32 v87, v87\n\t" \
    "v_exp_f32 v88, v88\n\tv_exp_f32 v89, v89\n\tv_smfmac_f32_32x32x32_bf16 %[o1], %[v0b], v[80:87], %[idx]\n\tv_exp_f32 v90, v90\n\t" \
    "v_exp_f32 v91, v91\n\tv_exp_f32 v92, v92\n\tv_exp_f32 v93, v93\n\tv_exp_f32 v94, v94\n\t" \
    "v_exp_f32 v95, v95\n\tv_exp_f32 v64, v64\n\tv_exp_f32 v65, v65\n\tv_smfmac_f32_32x32x32_bf16 %[o1], %[v1b], v[88:95], %[idx]\n\t" \
    "s_waitcnt lgkmcnt(0)\n\tv_exp_f32 v66, v66\n\tv_exp_f32 v67, v67\n\tv_exp_f32 v68, v68\n\t" \
    "v_mfma_f32_32x32x16_bf16 v[80:95], %[kb], %[q3], %[ci]\n\tds_read_b128 %[kb], %[ad] offset:20480\n\tv_exp_f32 v69, v69\n\tv_exp_f32 v70, v70\n\t" \
    "v_exp_f32 v71, v71\n\tv_exp_f32 v72, v72\n\tv_exp_f32 v73, v73\n\tv_smfmac_f32_32x32x32_bf16 %[o2], %[v0b], v[64:71], %[idx]\n\t" \
    "v_exp_f32 v74, v74\n\tv_exp_f32 v75, v75\n\tv_exp_f32 v76, v76\n\tv_exp_f32 v77, v77\n\t" \
    "v_exp_f32 v78, v78\n\tv_exp_f32 v79, v79\n\tv_exp_f32 v80, v80\n\tv_exp_f32 v81, v81\n\t" \
    "v_smfmac_f32_32x32x32_bf16 %[o2], %[v1b], v[72:79], %[idx]\n\tv_exp_f32 v82, v82\n\tv_exp_f32 v83, v83\n\tv_exp_f32 v84, v84\n\t" \
    "v_mfma_f32_32x32x16_bf16 v[64:79], %[ka], %[q0], %[ci]\n\tv_exp_f32 v85, v85\n\tv_exp_f32 v86, v86\n\tv_exp_f32 v87, v87\n\t" \
    "v_exp_f32 v88, v88\n\tv_exp_f32 v89, v89\n\tv_smfmac_f32_32x32x32_bf16 %[o3], %[v0b], v[80:87], %[idx]\n\tv_exp_f32 v90, v90\n\t" \
    "v_exp_f32 v91, v91\n\tv_exp_f32 v92, v92\n\tv_exp_f32 v93, v93\n\tv_exp_f32 v94, v94\n\t" \
    "v_exp_f32 v95, v95\n\tv_exp_f32 v64, v64\n\tv_exp_f32 v65, v65\n\tv_smfmac_f32_32x32x32_bf16 %[o3], %[v1b], v[88:95], %[idx]\n\t" \
    "s_mov_b64 exec, %[msk]\n\tds_read_b128 %[v1b], %[ad] offset:21760\n\tds_read_b128 %[v0b], %[ad] offset:21504\n\ts_mov_b64 exec, -1\n\t" \
    "v_exp_f32 v66, v66\n\tv_exp_f32 v67, v67\n\tv_exp_f32 v68, v68\n\tv_mfma_f32_32x32x16_bf16 v[80:95], %[ka], %[q1], %[ci]\n\t" \
    "v_exp_f32 v69, v69\n\tv_exp_f32 v70, v70\n\tv_exp_f32 v71, v71\n\tv_exp_f32 v72, v72\n\t" \
    "v_exp_f32 v73, v73\n\tv_smfmac_f32_32x32x32_bf16 %[o0], %[v0a], v[64:71], %[idx]\n\tv_exp_f32 v74, v74\n\tv_exp_f32 v75, v75\n\t" \
    "v_exp_f32 v76, v76\n\tv_exp_f32 v77, v77\n\tv_exp_f32 v78, v78\n\tv_exp_f32 v79, v79\n\t" \
    "v_exp_f32 v80, v80\n\tv_exp_f32 v81, v81\n\tv_smfmac_f32_32x32x32_bf16 %[o0], %[v1a], v[72:79], %[idx]\n\tv_exp_f32 v82, v82\n\t" \
    "v_exp_f32 v83, v83\n\tv_exp_f32 v84, v84\n\tv_mfma_f32_32x32x16_bf16 v[64:79], %[ka], %[q2], %[ci]\n\tv_exp_f32 v85, v85\n\t" \
    "v_exp_f32 v86, v86\n\tv_exp_f32 v87, v87\n\tv_exp_f32 v88, v88\n\tv_exp_f32 v89, v89\n\t" \
    "v_smfmac_f32_32x32x32_bf16 %[o1], %[v0a], v[80:87], %[idx]\n\tv_exp_f32 v90, v90\n\tv_exp_f32 v91, v91\n\tv_exp_f32 v92, v92\n\t" \
    "v_exp_f32 v93, v93\n\tv_exp_f32 v94, v94\n\tv_exp_f32 v95, v95\n\tv_exp_f32 v64, v64\n\t" \
    "v_exp_f32 v65, v65\n\tv_smfmac_f32_32x32x32_bf16 %[o1], %[v1a], v[88:95], %[idx]\n\ts_waitcnt lgkmcnt(0)\n\tv_exp_f32 v66, v66\n\t" \
    "v_exp_f32 v67, v67\n\tv_exp_f32 v68, v68\n\tv_mfma_f32_32x32x16_bf16 v[80:95], %[ka], %[q3], %[ci]\n\tds_read_b128 %[ka], %[ad] offset:24576\n\t" \
    "v_exp_f32 v69, v69\n\tv_exp_f32 v70, v70\n\tv_exp_f32 v71, v71\n\tv_exp_f32 v72, v72\n\t" \
    "v_exp_f32 v73, v73\n\tv_smfmac_f32_32x32x32_bf16 %[o2], %[v0a], v[64:71], %[idx]\n\tv_exp_f32 v74, v74\n\tv_exp_f32 v75, v75\n\t" \
    "v_exp_f32 v76, v76\n\tv_exp_f32 v77, v77\n\tv_exp_f32 v78, v78\n\tv_exp_f32 v79, v79\n\t" \
    "v_exp_f32 v80, v80\n\tv_exp_f32 v81, v81\n\tv_smfmac_f32_32x32x32_bf16 %[o2], %[v1a], v[72:79], %[idx]\n\tv_exp_f32 v82, v82\n\t" \
    "v_exp_f32 v83, v83\n\tv_exp_f32 v84, v84\n\tv_mfma_f32_32x32x16_bf16 v[64:79], %[kb], %[q0], %[ci]\n\tv_exp_f32 v85, v85\n\t" \
    "v_exp_f32 v86, v86\n\tv_exp_f32 v87, v87\n\tv_exp_f32 v88, v88\n\tv_exp_f32 v89, v89\n\t" \
    "v_smfmac_f32_32x32x32_bf16 %[o3], %[v0a], v[80:87], %[idx]\n\tv_exp_f32 v90, v90\n\tv_exp_f32 v91, v91\n\tv_exp_f32 v92, v92\n\t" \
    "v_exp_f32 v93, v93\n\tv_exp_f32 v94, v94\n\tv_exp_f32 v95, v95\n\tv_exp_f32 v64, v64\n\t" \
    "v_exp_f32 v65, v65\n\tv_smfmac_f32_32x32x32_bf16 %[o3], %[v1a], v[88:95], %[idx]\n\ts_mov_b64 exec, %[msk]\n\tds_read_b128 %[v1a], %[ad] offset:25856\n\t" \
    "ds_read_b128 %[v0a], %[ad] offset:25600\n\ts_mov_b64 exec, -1\n\tv_exp_f32 v66, v66\n\tv_exp_f32 v67, v67\n\t" \
    "v_exp_f32 v68, v68\n\tv_mfma_f32_32x32x16_bf16 v[80:95], %[kb], %[q1], %[ci]\n\tv_exp_f32 v69, v69\n\tv_exp_f32 v70, v70\n\t" \
    "v_exp_f32 v71, v71\n\tv_exp_f32 v72, v72\n\tv_exp_f32 v73, v73\n\tv_smfmac_f32_32x32x32_bf16 %[o0], %[v0b], v[64:71], %[idx]\n\t" \
    "v_exp_f32 v74, v74\n\tv_exp_f32 v75, v75\n\tv_exp_f32 v76, v76\n\tv_exp_f32 v77, v77\n\t" \
    "v_exp_f32 v78, v78\n\tv_exp_f32 v79, v79\n\tv_exp_f32 v80, v80\n\tv_exp_f32 v81, v81\n\t" \
    "v_smfmac_f32_32x32x32_bf16 %[o0], %[v1b], v[72:79], %[idx]\n\tv_exp_f32 v82, v82\n\tv_exp_f32 v83, v83\n\tv_exp_f32 v84, v84\n\t" \
    "v_mfma_f32_32x32x16_bf16 v[64:79], %[kb], %[q2], %[ci]\n\tv_exp_f32 v85, v85\n\tv_exp_f32 v86, v86\n\tv_exp_f32 v87, v87\n\t" \
    "v_exp_f32 v88, v88\n\tv_exp_f32 v89, v89\n\tv_smfmac_f32_32x32x32_bf16 %[o1], %[v0b], v[80:87], %[idx]\n\tv_exp_f32 v90, v90\n\t" \
    "v_exp_f32 v91, v91\n\tv_exp_f32 v92, v92\n\tv_exp_f32 v93, v93\n\tv_exp_f32 v94, v94\n\t" \
    "v_exp_f32 v95, v95\n\tv_exp_f32 v64, v64\n\tv_exp_f32 v65, v65\n\tv_smfmac_f32_32x32x32_bf16 %[o1], %[v1b], v[88:95], %[idx]\n\t" \
    "s_waitcnt lgkmcnt(0)\n\tv_exp_f32 v66, v66\n\tv_exp_f32 v67, v67\n\tv_exp_f32 v68, v68\n\t" \
    "v_mfma_f32_32x32x16_bf16 v[80:95], %[kb], %[q3], %[ci]\n\tds_read_b128 %[kb], %[ad] offset:28672\n\tv_exp_f32 v69, v69\n\tv_exp_f32 v70, v70\n\t" \
    "v_exp_f32 v71, v71\n\tv_exp_f32 v72, v72\n\tv_exp_f32 v73, v73\n\tv_smfmac_f32_32x32x32_bf16 %[o2], %[v0b], v[64:71], %[idx]\n\t" \
    "v_exp_f32 v74, v74\n\tv_exp_f32 v75, v75\n\tv_exp_f32 v76, v76\n\tv_exp_f32 v77, v77\n\t" \
    "v_exp_f32 v78, v78\n\tv_exp_f32 v79, v79\n\tv_exp_f32 v80, v80\n\tv_exp_f32 v81, v81\n\t" \
    "v_smfmac_f32_32x32x32_bf16 %[o2], %[v1b], v[72:79], %[idx]\n\tv_exp_f32 v82, v82\n\tv_exp_f32 v83, v83\n\tv_exp_f32 v84, v84\n\t" \
    "v_mfma_f32_32x32x16_bf16 v[64:79], %[ka], %[q0], %[ci]\n\tv_exp_f32 v85, v85\n\tv_exp_f32 v86, v86\n\tv_exp_f32 v87, v87\n\t" \
    "v_exp_f32 v88, v88\n\tv_exp_f32 v89, v89\n\tv_smfmac_f32_32x32x32_bf16 %[o3], %[v0b], v[80:87], %[idx]\n\ts_waitcnt vmcnt(0)\n\t" \
    "s_barrier\n\tv_exp_f32 v90, v90\n\tv_exp_f32 v91, v91\n\tv_exp_f32 v92, v92\n\t" \
    "v_exp_f32 v93, v93\n\tv_exp_f32 v94, v94\n\tv_exp_f32 v95, v95\n\tv_exp_f32 v64, v64\n\t" \
    "v_exp_f32 v65, v65\n\tv_smfmac_f32_32x32x32_bf16 %[o3], %[v1b], v[88:95], %[idx]\n\ts_mov_b64 exec, %[msk]\n\tds_read_b128 %[v1b], %[ad] offset:29952\n\t" \
    "ds_read_b128 %[v0b], %[ad] offset:29696\n\ts_mov_b64 exec, -1\n\tv_exp_f32 v66, v66\n\tv_exp_f32 v67, v67\n\t" \
    "v_exp_f32 v68, v68\n\tv_mfma_f32_32x32x16_bf16 v[80:95], %[ka], %[q1], %[ci]\n\tv_exp_f32 v69, v69\n\tv_exp_f32 v70, v70\n\t" \
    "v_exp_f32 v71, v71\n\tv_exp_f32 v72, v72\n\tv_exp_f32 v73, v73\n\tv_smfmac_f32_32x32x32_bf16 %[o0], %[v0a], v[64:71], %[idx]\n\t" \
    "v_exp_f32 v74, v74\n\tv_exp_f32 v75, v75\n\tv_exp_f32 v76, v76\n\tv_exp_f32 v77, v77\n\t" \
    "v_exp_f32 v78, v78\n\tv_exp_f32 v79, v79\n\tv_exp_f32 v80, v80\n\tv_exp_f32 v81, v81\n\t" \
    "v_smfmac_f32_32x32x32_bf16 %[o0], %[v1a], v[72:79], %[idx]\n\tv_exp_f32 v82, v82\n\tv_exp_f32 v83, v83\n\tv_exp_f32 v84, v84\n\t" \
    "v_mfma_f32_32x32x16_bf16 v[64:79], %[ka], %[q2], %[ci]\n\tv_exp_f32 v85, v85\n\tv_exp_f32 v86, v86\n\tv_exp_f32 v87, v87\n\t" \
    "v_exp_f32 v88, v88\n\tv_exp_f32 v89, v89\n\tv_smfmac_f32_32x32x32_bf16 %[o1], %[v0a], v[80:87], %[idx]\n\tv_exp_f32 v90, v90\n\t" \
    "v_exp_f32 v91, v91\n\tv_exp_f32 v92, v92\n\tv_exp_f32 v93, v93\n\tv_exp_f32 v94, v94\n\t" \
    "v_exp_f32 v95, v95\n\tv_exp_f32 v64, v64\n\tv_exp_f32 v65, v65\n\tv_smfmac_f32_32x32x32_bf16 %[o1], %[v1a], v[88:95], %[idx]\n\t" \
    "s_waitcnt lgkmcnt(0)\n\tv_exp_f32 v66, v66\n\tv_exp_f32 v67, v67\n\tv_exp_f32 v68, v68\n\t" \
    "v_mfma_f32_32x32x16_bf16 v[80:95], %[ka], %[q3], %[ci]\n\tds_read_b128 %[ka], %[ad] offset:32768\n\tv_exp_f32 v69, v69\n\tv_exp_f32 v70, v70\n\t" \
    "v_exp_f32 v71, v71\n\tv_exp_f32 v72, v72\n\tv_exp_f32 v73, v73\n\tv_smfmac_f32_32x32x32_bf16 %[o2], %[v0a], v[64:71], %[idx]\n\t" \
    "v_exp_f32 v74, v74\n\tv_exp_f32 v75, v75\n\tv_exp_f32 v76, v76\n\tv_exp_f32 v77, v77\n\t" \
    "v_exp_f32 v78, v78\n\tv_exp_f32 v79, v79\n\tv_exp_f32 v80, v80\n\tv_exp_f32 v81, v81\n\t" \
    "v_smfmac_f32_32x32x32_bf16 %[o2], %[v1a], v[72:79], %[idx]\n\tv_exp_f32 v82, v82\n\tv_exp_f32 v83, v83\n\tv_exp_f32 v84, v84\n\t" \
    "v_mfma_f32_32x32x16_bf16 v[64:79], %[kb], %[q0], %[ci]\n\tv_exp_f32 v85, v85\n\tv_exp_f32 v86, v86\n\tv_exp_f32 v87, v87\n\t" \
    "v_exp_f32 v88, v88\n\tv_exp_f32 v89, v89\n\tv_smfmac_f32_32x32x32_bf16 %[o3], %[v0a], v[80:87], %[idx]\n\tv_exp_f32 v90, v90\n\t" \
    "v_exp_f32 v91, v91\n\tv_exp_f32 v92, v92\n\tv_exp_f32 v93, v93\n\tv_exp_f32 v94, v94\n\t" \
    "v_exp_f32 v95, v95\n\tv_exp_f32 v64, v64\n\tv_exp_f32 v65, v65\n\tv_smfmac_f32_32x32x32_bf16 %[o3], %[v1a], v[88:95], %[idx]\n\t" \
    "s_mov_b64 exec, %[msk]\n\tds_read_b128 %[v1a], %[ad] offset:34048\n\tds_read_b128 %[v0a], %[ad] offset:33792\n\ts_mov_b64 exec, -1\n\t" \
    "v_exp_f32 v66, v66\n\tv_exp_f32 v67, v67\n\tv_exp_f32 v68, v68\n\tv_mfma_f32_32x32x16_bf16 v[80:95], %[kb], %[q1], %[ci]\n\t" \
    "v_exp_f32 v69, v69\n\tv_exp_f32 v70, v70\n\tv_exp_f32 v71, v71\n\tv_exp_f32 v72, v72\n\t" \
    "v_exp_f32 v73, v73\n\tv_smfmac_f32_32x32x32_bf16 %[o0], %[v0b], v[64:71], %[idx]\n\tv_exp_f32 v74, v74\n\tv_exp_f32 v75, v75\n\t" \
    "v_exp_f32 v76, v76\n\tv_exp_f32 v77, v77\n\tv_exp_f32 v78, v78\n\tv_exp_f32 v79, v79\n\t" \
    "v_exp_f32 v80, v80\n\tv_exp_f32 v81, v81\n\tv_smfmac_f32_32x32x32_bf16 %[o0], %[v1b], v[72:79], %[idx]\n\tv_exp_f32 v82, v82\n\t" \
    "v_exp_f32 v83, v83\n\tv_exp_f32 v84, v84\n\tv_mfma_f32_32x32x16_bf16 v[64:79], %[kb], %[q2], %[ci]\n\tv_exp_f32 v85, v85\n\t" \
    "v_exp_f32 v86, v86\n\tv_exp_f32 v87, v87\n\tv_exp_f32 v88, v88\n\tv_exp_f32 v89, v89\n\t" \
    "v_smfmac_f32_32x32x32_bf16 %[o1], %[v0b], v[80:87], %[idx]\n\tv_exp_f32 v90, v90\n\tv_exp_f32 v91, v91\n\tv_exp_f32 v92, v92\n\t" \
    "v_exp_f32 v93, v93\n\tv_exp_f32 v94, v94\n\tv_exp_f32 v95, v95\n\tv_exp_f32 v64, v64\n\t" \
    "v_exp_f32 v65, v65\n\tv_smfmac_f32_32x32x32_bf16 %[o1], %[v1b], v[88:95], %[idx]\n\ts_waitcnt lgkmcnt(0)\n\tv_exp_f32 v66, v66\n\t" \
    "v_exp_f32 v67, v67\n\tv_exp_f32 v68, v68\n\tv_mfma_f32_32x32x16_bf16 v[80:95], %[kb], %[q3], %[ci]\n\tds_read_b128 %[kb], %[ad] offset:36864\n\t" \
    "v_exp_f32 v69, v69\n\tv_exp_f32 v70, v70\n\tv_exp_f32 v71, v71\n\tv_exp_f32 v72, v72\n\t" \
    "v_exp_f32 v73, v73\n\tv_smfmac_f32_32x32x32_bf16 %[o2], %[v0b], v[64:71], %[idx]\n\tv_exp_f32 v74, v74\n\tv_exp_f32 v75, v75\n\t" \
    "v_exp_f32 v76, v76\n\tv_exp_f32 v77, v77\n\tv_exp_f32 v78, v78\n\tv_exp_f32 v79, v79\n\t" \
    "v_exp_f32 v80, v80\n\tv_exp_f32 v81, v81\n\tv_smfmac_f32_32x32x32_bf16 %[o2], %[v1b], v[72:79], %[idx]\n\tv_exp_f32 v82, v82\n\t" \
    "v_exp_f32 v83, v83\n\tv_exp_f32 v84, v84\n\tv_mfma_f32_32x32x16_bf16 v[64:79], %[ka], %[q0], %[ci]\n\tv_exp_f32 v85, v85\n\t" \
    "v_exp_f32 v86, v86\n\tv_exp_f32 v87, v87\n\tv_exp_f32 v88, v88\n\tv_exp_f32 v89, v89\n\t" \
    "v_smfmac_f32_32x32x32_bf16 %[o3], %[v0b], v[80:87], %[idx]\n\tv_add_u32 %[ad], 0x8000, %[ad]\n\tv_exp_f32 v90, v90\n\tv_exp_f32 v91, v91\n\t" \
    "v_exp_f32 v92, v92\n\tv_exp_f32 v93, v93\n\tv_exp_f32 v94, v94\n\tv_exp_f32 v95, v95\n\t" \
    "v_exp_f32 v64, v64\n\tv_exp_f32 v65, v65\n\tv_smfmac_f32_32x32x32_bf16 %[o3], %[v1b], v[88:95], %[idx]\n\ts_mov_b64 exec, %[msk]\n\t" \
    "ds_read_b128 %[v1b], %[ad] offset:5376\n\tds_read_b128 %[v0b], %[ad] offset:5120\n\ts_mov_b64 exec, -1\n\tv_exp_f32 v66, v66\n\t" \
    "v_exp_f32 v67, v67\n\tv_exp_f32 v68, v68\n\tv_mfma_f32_32x32x16_bf16 v[80:95], %[ka], %[q1], %[ci]\n\tv_exp_f32 v69, v69\n\t" \
    "v_exp_f32 v70, v70\n\tv_exp_f32 v71, v71\n\tv_exp_f32 v72, v72\n\tv_exp_f32 v73, v73\n\t" \
    "v_smfmac_f32_32x32x32_bf16 %[o0], %[v0a], v[64:71], %[idx]\n\tv_exp_f32 v74, v74\n\tv_exp_f32 v75, v75\n\tv_exp_f32 v76, v76\n\t" \
    "v_exp_f32 v77, v77\n\tv_exp_f32 v78, v78\n\tv_exp_f32 v79, v79\n\tv_exp_f32 v80, v80\n\t" \
    "v_exp_f32 v81, v81\n\tv_smfmac_f32_32x32x32_bf16 %[o0], %[v1a], v[72:79], %[idx]\n\tv_exp_f32 v82, v82\n\tv_exp_f32 v83, v83\n\t" \
    "v_exp_f32 v84, v84\n\tv_mfma_f32_32x32x16_bf16 v[64:79], %[ka], %[q2], %[ci]\n\tv_exp_f32 v85, v85\n\tv_exp_f32 v86, v86\n\t" \
    "v_exp_f32 v87, v87\n\tv_exp_f32 v88, v88\n\tv_exp_f32 v89, v89\n\tv_smfmac_f32_32x32x32_bf16 %[o1], %[v0a], v[80:87], %[idx]\n\t" \
    "v_exp_f32 v90, v90\n\tv_exp_f32 v91, v91\n\tv_exp_f32 v92, v92\n\tv_exp_f32 v93, v93\n\t" \
    "v_exp_f32 v94, v94\n\tv_exp_f32 v95, v95\n\tv_exp_f32 v64, v64\n\tv_exp_f32 v65, v65\n\t" \
    "v_smfmac_f32_32x32x32_bf16 %[o1], %[v1a], v[88:95], %[idx]\n\ts_waitcnt lgkmcnt(0)\n\tv_exp_f32 v66, v66\n\tv_exp_f32 v67, v67\n\t" \
    "v_exp_f32 v68, v68\n\tv_mfma_f32_32x32x16_bf16 v[80:95], %[ka], %[q3], %[ci]\n\tds_read_b128 %[ka], %[ad] offset:8192\n\tv_exp_f32 v69, v69\n\t" \
    "v_exp_f32 v70, v70\n\tv_exp_f32 v71, v71\n\tv_exp_f32 v72, v72\n\tv_exp_f32 v73, v73\n\t" \
    "v_smfmac_f32_32x32x32_bf16 %[o2], %[v0a], v[64:71], %[idx]\n\tv_exp_f32 v74, v74\n\tv_exp_f32 v75, v75\n\tv_exp_f32 v76, v76\n\t" \
    "v_exp_f32 v77, v77\n\tv_exp_f32 v78, v78\n\tv_exp_f32 v79, v79\n\tv_exp_f32 v80, v80\n\t" \
    "v_exp_f32 v81, v81\n\tv_smfmac_f32_32x32x32_bf16 %[o2], %[v1a], v[72:79], %[idx]\n\tv_exp_f32 v82, v82\n\tv_exp_f32 v83, v83\n\t" \
    "v_exp_f32 v84, v84\n\tv_mfma_f32_32x32x16_bf16 v[64:79], %[kb], %[q0], %[ci]\n\tv_exp_f32 v85, v85\n\tv_exp_f32 v86, v86\n\t" \
    "v_exp_f32 v87, v87\n\tv_exp_f32 v88, v88\n\tv_exp_f32 v89, v89\n\tv_smfmac_f32_32x32x32_bf16 %[o3], %[v0a], v[80:87], %[idx]\n\t" \
    "v_exp_f32 v90, v90\n\tv_exp_f32 v91, v91\n\tv_exp_f32 v92, v92\n\tv_exp_f32 v93, v93\n\t" \
    "v_exp_f32 v94, v94\n\tv_exp_f32 v95, v95\n\tv_exp_f32 v64, v64\n\tv_exp_f32 v65, v65\n\t" \
    "v_smfmac_f32_32x32x32_bf16 %[o3], %[v1a], v[88:95], %[idx]\n\ts_mov_b64 exec, %[msk]\n\tds_read_b128 %[v1a], %[ad] offset:9472\n\tds_read_b128 %[v0a], %[ad] offset:9216\n\t" \
    "s_mov_b64 exec, -1\n\tv_exp_f32 v66, v66\n\tv_exp_f32 v67, v67\n\tv_exp_f32 v68, v68\n\t" \
    "v_mfma_f32_32x32x16_bf16 v[80:95], %[kb], %[q1], %[ci]\n\tv_exp_f32 v69, v69\n\tv_exp_f32 v70, v70\n\tv_exp_f32 v71, v71\n\t" \
    "v_exp_f32 v72, v72\n\tv_exp_f32 v73, v73\n\tv_smfmac_f32_32x32x32_bf16 %[o0], %[v0b], v[64:71], %[idx]\n\tv_exp_f32 v74, v74\n\t" \
    "v_exp_f32 v75, v75\n\tv_exp_f32 v76, v76\n\tv_exp_f32 v77, v77\n\tv_exp_f32 v78, v78\n\t" \
    "v_exp_f32 v79, v79\n\tv_exp_f32 v80, v80\n\tv_exp_f32 v81, v81\n\tv_smfmac_f32_32x32x32_bf16 %[o0], %[v1b], v[72:79], %[idx]\n\t" \
    "v_exp_f32 v82, v82\n\tv_exp_f32 v83, v83\n\tv_exp_f32 v84, v84\n\tv_mfma_f32_32x32x16_bf16 v[64:79], %[kb], %[q2], %[ci]\n\t" \
    "v_exp_f32 v85, v85\n\tv_exp_f32 v86, v86\n\tv_exp_f32 v87, v87\n\tv_exp_f32 v88, v88\n\t" \
    "v_exp_f32 v89, v89\n\tv_smfmac_f32_32x32x32_bf16 %[o1], %[v0b], v[80:87], %[idx]\n\tv_exp_f32 v90, v90\n\tv_exp_f32 v91, v91\n\t" \
    "v_exp_f32 v92, v92\n\tv_exp_f32 v93, v93\n\tv_exp_f32 v94, v94\n\tv_exp_f32 v95, v95\n\t" \
    "v_exp_f32 v64, v64\n\tv_exp_f32 v65, v65\n\tv_smfmac_f32_32x32x32_bf16 %[o1], %[v1b], v[88:95], %[idx]\n\ts_waitcnt lgkmcnt(0)\n\t" \
    "v_exp_f32 v66, v66\n\tv_exp_f32 v67, v67\n\tv_exp_f32 v68, v68\n\tv_mfma_f32_32x32x16_bf16 v[80:95], %[kb], %[q3], %[ci]\n\t" \
    "ds_read_b128 %[kb], %[ad] offset:12288\n\tv_exp_f32 v69, v69\n\tv_exp_f32 v70, v70\n\tv_exp_f32 v71, v71\n\t" \
    "v_exp_f32 v72, v72\n\tv_exp_f32 v73, v73\n\tv_smfmac_f32_32x32x32_bf16 %[o2], %[v0b], v[64:71], %[idx]\n\tv_exp_f32 v74, v74\n\t" \
    "v_exp_f32 v75, v75\n\tv_exp_f32 v76, v76\n\tv_exp_f32 v77, v77\n\tv_exp_f32 v78, v78\n\t" \
    "v_exp_f32 v79, v79\n\tv_exp_f32 v80, v80\n\tv_exp_f32 v81, v81\n\tv_smfmac_f32_32x32x32_bf16 %[o2], %[v1b], v[72:79], %[idx]\n\t" \
    "v_exp_f32 v82, v82\n\tv_exp_f32 v83, v83\n\tv_exp_f32 v84, v84\n\tv_mfma_f32_32x32x16_bf16 v[64:79], %[ka], %[q0], %[ci]\n\t" \
    "v_exp_f32 v85, v85\n\tv_exp_f32 v86, v86\n\tv_exp_f32 v87, v87\n\tv_exp_f32 v88, v88\n\t" \
    "v_exp_f32 v89, v89\n\tv_smfmac_f32_32x32x32_bf16 %[o3], %[v0b], v[80:87], %[idx]\n\tv_exp_f32 v90, v90\n\tv_exp_f32 v91, v91\n\t" \
    "v_exp_f32 v92, v92\n\tv_exp_f32 v93, v93\n\tv_exp_f32 v94, v94\n\tv_exp_f32 v95, v95\n\t" \
    "v_exp_f32 v64, v64\n\tv_exp_f32 v65, v65\n\tv_smfmac_f32_32x32x32_bf16 %[o3], %[v1b], v[88:95], %[idx]\n\ts_mov_b64 exec, %[msk]\n\t" \
    "ds_read_b128 %[v1b], %[ad] offset:13568\n\tds_read_b128 %[v0b], %[ad] offset:13312\n\ts_mov_b64 exec, -1\n\tv_exp_f32 v66, v66\n\t" \
    "v_exp_f32 v67, v67\n\tv_exp_f32 v68, v68\n\tv_mfma_f32_32x32x16_bf16 v[80:95], %[ka], %[q1], %[ci]\n\tv_exp_f32 v69, v69\n\t" \
    "v_exp_f32 v70, v70\n\tv_exp_f32 v71, v71\n\tv_exp_f32 v72, v72\n\tv_exp_f32 v73, v73\n\t" \
    "v_smfmac_f32_32x32x32_bf16 %[o0], %[v0a], v[64:71], %[idx]\n\tv_exp_f32 v74, v74\n\tv_exp_f32 v75, v75\n\tv_exp_f32 v76, v76\n\t" \
    "v_exp_f32 v77, v77\n\tv_exp_f32 v78, v78\n\tv_exp_f32 v79, v79\n\tv_exp_f32 v80, v80\n\t" \
    "v_exp_f32 v81, v81\n\tv_smfmac_f32_32x32x32_bf16 %[o0], %[v1a], v[72:79], %[idx]\n\tv_exp_f32 v82, v82\n\tv_exp_f32 v83, v83\n\t" \
    "v_exp_f32 v84, v84\n\tv_mfma_f32_32x32x16_bf16 v[64:79], %[ka], %[q2], %[ci]\n\tv_exp_f32 v85, v85\n\tv_exp_f32 v86, v86\n\t" \
    "v_exp_f32 v87, v87\n\tv_exp_f32 v88, v88\n\tv_exp_f32 v89, v89\n\tv_smfmac_f32_32x32x32_bf16 %[o1], %[v0a], v[80:87], %[idx]\n\t" \
    "v_exp_f32 v90, v90\n\tv_exp_f32 v91, v91\n\tv_exp_f32 v92, v92\n\tv_exp_f32 v93, v93\n\t" \
    "v_exp_f32 v94, v94\n\tv_exp_f32 v95, v95\n\tv_exp_f32 v64, v64\n\tv_exp_f32 v65, v65\n\t" \
    "v_smfmac_f32_32x32x32_bf16 %[o1], %[v1a], v[88:95], %[idx]\n\ts_waitcnt lgkmcnt(0)\n\tv_exp_f32 v66, v66\n\tv_exp_f32 v67, v67\n\t" \
    "v_exp_f32 v68, v68\n\tv_mfma_f32_32x32x16_bf16 v[80:95], %[ka], %[q3], %[ci]\n\tds_read_b128 %[ka], %[ad] offset:16384\n\tv_exp_f32 v69, v69\n\t" \
    "v_exp_f32 v70, v70\n\tv_exp_f32 v71, v71\n\tv_exp_f32 v72, v72\n\tv_exp_f32 v73, v73\n\t" \
    "v_smfmac_f32_32x32x32_bf16 %[o2], %[v0a], v[64:71], %[idx]\n\tv_exp_f32 v74, v74\n\tv_exp_f32 v75, v75\n\tv_exp_f32 v76, v76\n\t" \
    "v_exp_f32 v77, v77\n\tv_exp_f32 v78, v78\n\tv_exp_f32 v79, v79\n\tv_exp_f32 v80, v80\n\t" \
    "v_exp_f32 v81, v81\n\tv_smfmac_f32_32x32x32_bf16 %[o2], %[v1a], v[72:79], %[idx]\n\tv_exp_f32 v82, v82\n\tv_exp_f32 v83, v83\n\t" \
    "v_exp_f32 v84, v84\n\tv_mfma_f32_32x32x16_bf16 v[64:79], %[kb], %[q0], %[ci]\n\tv_exp_f32 v85, v85\n\tv_exp_f32 v86, v86\n\t" \
    "v_exp_f32 v87, v87\n\tv_exp_f32 v88, v88\n\tv_exp_f32 v89, v89\n\tv_smfmac_f32_32x32x32_bf16 %[o3], %[v0a], v[80:87], %[idx]\n\t" \
    "v_exp_f32 v90, v90\n\tv_exp_f32 v91, v91\n\tv_exp_f32 v92, v92\n\tv_exp_f32 v93, v93\n\t" \
    "v_exp_f32 v94, v94\n\tv_exp_f32 v95, v95\n\tv_exp_f32 v64, v64\n\tv_exp_f32 v65, v65\n\t" \
    "v_smfmac_f32_32x32x32_bf16 %[o3], %[v1a], v[88:95], %[idx]\n\ts_mov_b64 exec, %[msk]\n\tds_read_b128 %[v1a], %[ad] offset:17664\n\tds_read_b128 %[v0a], %[ad] offset:17408\n\t" \
    "s_mov_b64 exec, -1\n\tv_exp_f32 v66, v66\n\tv_exp_f32 v67, v67\n\tv_exp_f32 v68, v68\n\t" \
    "v_mfma_f32_32x32x16_bf16 v[80:95], %[kb], %[q1], %[ci]\n\tv_exp_f32 v69, v69\n\tv_exp_f32 v70, v70\n\tv_exp_f32 v71, v71\n\t" \
    "v_exp_f32 v72, v72\n\tv_exp_f32 v73, v73\n\tv_smfmac_f32_32x32x32_bf16 %[o0], %[v0b], v[64:71], %[idx]\n\tv_exp_f32 v74, v74\n\t" \
    "v_exp_f32 v75, v75\n\tv_exp_f32 v76, v76\n\tv_exp_f32 v77, v77\n\tv_exp_f32 v78, v78\n\t" \
    "v_exp_f32 v79, v79\n\tv_exp_f32 v80, v80\n\tv_exp_f32 v81, v81\n\tv_smfmac_f32_32x32x32_bf16 %[o0], %[v1b], v[72:79], %[idx]\n\t" \
    "v_exp_f32 v82, v82\n\tv_exp_f32 v83, v83\n\tv_exp_f32 v84, v84\n\tv_mfma_f32_32x32x16_bf16 v[64:79], %[kb], %[q2], %[ci]\n\t" \
    "v_exp_f32 v85, v85\n\tv_exp_f32 v86, v86\n\tv_exp_f32 v87, v87\n\tv_exp_f32 v88, v88\n\t" \
    "v_exp_f32 v89, v89\n\tv_smfmac_f32_32x32x32_bf16 %[o1], %[v0b], v[80:87], %[idx]\n\tv_exp_f32 v90, v90\n\tv_exp_f32 v91, v91\n\t" \
    "v_exp_f32 v92, v92\n\tv_exp_f32 v93, v93\n\tv_exp_f32 v94, v94\n\tv_exp_f32 v95, v95\n\t" \
    "v_exp_f32 v64, v64\n\tv_exp_f32 v65, v65\n\tv_smfmac_f32_32x32x32_bf16 %[o1], %[v1b], v[88:95], %[idx]\n\ts_waitcnt lgkmcnt(0)\n\t" \
    "v_exp_f32 v66, v66\n\tv_exp_f32 v67, v67\n\tv_exp_f32 v68, v68\n\tv_mfma_f32_32x32x16_bf16 v[80:95], %[kb], %[q3], %[ci]\n\t" \
    "ds_read_b128 %[kb], %[ad] offset:20480\n\tv_exp_f32 v69, v69\n\tv_exp_f32 v70, v70\n\tv_exp_f32 v71, v71\n\t" \
    "v_exp_f32 v72, v72\n\tv_exp_f32 v73, v73\n\tv_smfmac_f32_32x32x32_bf16 %[o2], %[v0b], v[64:71], %[idx]\n\tv_exp_f32 v74, v74\n\t" \
    "v_exp_f32 v75, v75\n\tv_exp_f32 v76, v76\n\tv_exp_f32 v77, v77\n\tv_exp_f32 v78, v78\n\t" \
    "v_exp_f32 v79, v79\n\tv_exp_f32 v80, v80\n\tv_exp_f32 v81, v81\n\tv_smfmac_f32_32x32x32_bf16 %[o2], %[v1b], v[72:79], %[idx]\n\t" \
    "v_exp_f32 v82, v82\n\tv_exp_f32 v83, v83\n\tv_exp_f32 v84, v84\n\tv_mfma_f32_32x32x16_bf16 v[64:79], %[ka], %[q0], %[ci]\n\t" \
    "v_exp_f32 v85, v85\n\tv_exp_f32 v86, v86\n\tv_exp_f32 v87, v87\n\tv_exp_f32 v88, v88\n\t" \
    "v_exp_f32 v89, v89\n\tv_smfmac_f32_32x32x32_bf16 %[o3], %[v0b], v[80:87], %[idx]\n\tv_exp_f32 v90, v90\n\tv_exp_f32 v91, v91\n\t" \
    "v_exp_f32 v92, v92\n\tv_exp_f32 v93, v93\n\tv_exp_f32 v94, v94\n\tv_exp_f32 v95, v95\n\t" \
    "v_exp_f32 v64, v64\n\tv_exp_f32 v65, v65\n\tv_smfmac_f32_32x32x32_bf16 %[o3], %[v1b], v[88:95], %[idx]\n\ts_mov_b64 exec, %[msk]\n\t" \
    "ds_read_b128 %[v1b], %[ad] offset:21760\n\tds_read_b128 %[v0b], %[ad] offset:21504\n\ts_mov_b64 exec, -1\n\tv_exp_f32 v66, v66\n\t" \
    "v_exp_f32 v67, v67\n\tv_exp_f32 v68, v68\n\tv_mfma_f32_32x32x16_bf16 v[80:95], %[ka], %[q1], %[ci]\n\tv_exp_f32 v69, v69\n\t" \
    "v_exp_f32 v70, v70\n\tv_exp_f32 v71, v71\n\tv_exp_f32 v72, v72\n\tv_exp_f32 v73, v73\n\t" \
    "v_smfmac_f32_32x32x32_bf16 %[o0], %[v0a], v[64:71], %[idx]\n\tv_exp_f32 v74, v74\n\tv_exp_f32 v75, v75\n\tv_exp_f32 v76, v76\n\t" \
    "v_exp_f32 v77, v77\n\tv_exp_f32 v78, v78\n\tv_exp_f32 v79, v79\n\tv_exp_f32 v80, v80\n\t" \
    "v_exp_f32 v81, v81\n\tv_smfmac_f32_32x32x32_bf16 %[o0], %[v1a], v[72:79], %[idx]\n\tv_exp_f32 v82, v82\n\tv_exp_f32 v83, v83\n\t" \
    "v_exp_f32 v84, v84\n\tv_mfma_f32_32x32x16_bf16 v[64:79], %[ka], %[q2], %[ci]\n\tv_exp_f32 v85, v85\n\tv_exp_f32 v86, v86\n\t" \
    "v_exp_f32 v87, v87\n\tv_exp_f32 v88, v88\n\tv_exp_f32 v89, v89\n\tv_smfmac_f32_32x32x32_bf16 %[o1], %[v0a], v[80:87], %[idx]\n\t" \
    "v_exp_f32 v90, v90\n\tv_exp_f32 v91, v91\n\tv_exp_f32 v92, v92\n\tv_exp_f32 v93, v93\n\t" \
    "v_exp_f32 v94, v94\n\tv_exp_f32 v95, v95\n\tv_exp_f32 v64, v64\n\tv_exp_f32 v65, v65\n\t" \
    "v_smfmac_f32_32x32x32_bf16 %[o1], %[v1a], v[88:95], %[idx]\n\ts_waitcnt lgkmcnt(0)\n\tv_exp_f32 v66, v66\n\tv_exp_f32 v67, v67\n\t" \
    "v_exp_f32 v68, v68\n\tv_mfma_f32_32x32x16_bf16 v[80:95], %[ka], %[q3], %[ci]\n\tds_read_b128 %[ka], %[ad] offset:24576\n\tv_exp_f32 v69, v69\n\t" \
    "v_exp_f32 v70, v70\n\tv_exp_f32 v71, v71\n\tv_exp_f32 v72, v72\n\tv_exp_f32 v73, v73\n\t" \
    "v_smfmac_f32_32x32x32_bf16 %[o2], %[v0a], v[64:71], %[idx]\n\tv_exp_f32 v74, v74\n\tv_exp_f32 v75, v75\n\tv_exp_f32 v76, v76\n\t" \
    "v_exp_f32 v77, v77\n\tv_exp_f32 v78, v78\n\tv_exp_f32 v79, v79\n\tv_exp_f32 v80, v80\n\t" \
    "v_exp_f32 v81, v81\n\tv_smfmac_f32_32x32x32_bf16 %[o2], %[v1a], v[72:79], %[idx]\n\tv_exp_f32 v82, v82\n\tv_exp_f32 v83, v83\n\t" \
    "v_exp_f32 v84, v84\n\tv_mfma_f32_32x32x16_bf16 v[64:79], %[kb], %[q0], %[ci]\n\tv_exp_f32 v85, v85\n\tv_exp_f32 v86, v86\n\t" \
    "v_exp_f32 v87, v87\n\tv_exp_f32 v88, v88\n\tv_exp_f32 v89, v89\n\tv_smfmac_f32_32x32x32_bf16 %[o3], %[v0a], v[80:87], %[idx]\n\t" \
    "v_exp_f32 v90, v90\n\tv_exp_f32 v91, v91\n\tv_exp_f32 v92, v92\n\tv_exp_f32 v93, v93\n\t" \
    "v_exp_f32 v94, v94\n\tv_exp_f32 v95, v95\n\tv_exp_f32 v64, v64\n\tv_exp_f32 v65, v65\n\t" \
    "v_smfmac_f32_32x32x32_bf16 %[o3], %[v1a], v[88:95], %[idx]\n\ts_mov_b64 exec, %[msk]\n\tds_read_b128 %[v1a], %[ad] offset:25856\n\tds_read_b128 %[v0a], %[ad] offset:25600\n\t" \
    "s_mov_b64 exec, -1\n\tv_exp_f32 v66, v66\n\tv_exp_f32 v67, v67\n\tv_exp_f32 v68, v68\n\t" \
    "v_mfma_f32_32x32x16_bf16 v[80:95], %[kb], %[q1], %[ci]\n\tv_exp_f32 v69, v69\n\tv_exp_f32 v70, v70\n\tv_exp_f32 v71, v71\n\t" \
    "v_exp_f32 v72, v72\n\tv_exp_f32 v73, v73\n\tv_smfmac_f32_32x32x32_bf16 %[o0], %[v0b], v[64:71], %[idx]\n\tv_exp_f32 v74, v74\n\t" \
    "v_exp_f32 v75, v75\n\tv_exp_f32 v76, v76\n\tv_exp_f32 v77, v77\n\tv_exp_f32 v78, v78\n\t" \
    "v_exp_f32 v79, v79\n\tv_exp_f32 v80, v80\n\tv_exp_f32 v81, v81\n\tv_smfmac_f32_32x32x32_bf16 %[o0], %[v1b], v[72:79], %[idx]\n\t" \
    "v_exp_f32 v82, v82\n\tv_exp_f32 v83, v83\n\tv_exp_f32 v84, v84\n\tv_mfma_f32_32x32x16_bf16 v[64:79], %[kb], %[q2], %[ci]\n\t" \
    "v_exp_f32 v85, v85\n\tv_exp_f32 v86, v86\n\tv_exp_f32 v87, v87\n\tv_exp_f32 v88, v88\n\t" \
    "v_exp_f32 v89, v89\n\tv_smfmac_f32_32x32x32_bf16 %[o1], %[v0b], v[80:87], %[idx]\n\tv_exp_f32 v90, v90\n\tv_exp_f32 v91, v91\n\t" \
    "v_exp_f32 v92, v92\n\tv_exp_f32 v93, v93\n\tv_exp_f32 v94, v94\n\tv_exp_f32 v95, v95\n\t" \
    "v_exp_f32 v64, v64\n\tv_exp_f32 v65, v65\n\tv_smfmac_f32_32x32x32_bf16 %[o1], %[v1b], v[88:95], %[idx]\n\ts_waitcnt lgkmcnt(0)\n\t" \
    "v_exp_f32 v66, v66\n\tv_exp_f32 v67, v67\n\tv_exp_f32 v68, v68\n\tv_mfma_f32_32x32x16_bf16 v[80:95], %[kb], %[q3], %[ci]\n\t" \
    "ds_read_b128 %[kb], %[ad] offset:28672\n\tv_exp_f32 v69, v69\n\tv_exp_f32 v70, v70\n\tv_exp_f32 v71, v71\n\t" \
    "v_exp_f32 v72, v72\n\tv_exp_f32 v73, v73\n\tv_smfmac_f32_32x32x32_bf16 %[o2], %[v0b], v[64:71], %[idx]\n\tv_exp_f32 v74, v74\n\t" \
    "v_exp_f32 v75, v75\n\tv_exp_f32 v76, v76\n\tv_exp_f32 v77, v77\n\tv_exp_f32 v78, v78\n\t" \
    "v_exp_f32 v79, v79\n\tv_exp_f32 v80, v80\n\tv_exp_f32 v81, v81\n\tv_smfmac_f32_32x32x32_bf16 %[o2], %[v1b], v[72:79], %[idx]\n\t" \
    "v_exp_f32 v82, v82\n\tv_exp_f32 v83, v83\n\tv_exp_f32 v84, v84\n\tv_mfma_f32_32x32x16_bf16 v[64:79], %[ka], %[q0], %[ci]\n\t" \
    "v_exp_f32 v85, v85\n\tv_exp_f32 v86, v86\n\tv_exp_f32 v87, v87\n\tv_exp_f32 v88, v88\n\t" \
    "v_exp_f32 v89, v89\n\tv_smfmac_f32_32x32x32_bf16 %[o3], %[v0b], v[80:87], %[idx]\n\ts_barrier\n\tv_exp_f32 v90, v90\n\t" \
    "v_exp_f32 v91, v91\n\tv_exp_f32 v92, v92\n\tv_exp_f32 v93, v93\n\tv_exp_f32 v94, v94\n\t" \
    "v_exp_f32 v95, v95\n\tv_exp_f32 v64, v64\n\tv_exp_f32 v65, v65\n\tv_smfmac_f32_32x32x32_bf16 %[o3], %[v1b], v[88:95], %[idx]\n\t" \
    "s_mov_b64 exec, %[msk]\n\tds_read_b128 %[v1b], %[ad] offset:29952\n\tds_read_b128 %[v0b], %[ad] offset:29696\n\ts_mov_b64 exec, -1\n\t" \
    "v_exp_f32 v66, v66\n\tv_exp_f32 v67, v67\n\tv_exp_f32 v68, v68\n\tv_mfma_f32_32x32x16_bf16 v[80:95], %[ka], %[q1], %[ci]\n\t" \
    "v_exp_f32 v69, v69\n\tv_exp_f32 v70, v70\n\tv_exp_f32 v71, v71\n\tv_exp_f32 v72, v72\n\t" \
    "v_exp_f32 v73, v73\n\tv_smfmac_f32_32x32x32_bf16 %[o0], %[v0a], v[64:71], %[idx]\n\tv_exp_f32 v74, v74\n\tv_exp_f32 v75, v75\n\t" \
    "v_exp_f32 v76, v76\n\tv_exp_f32 v77, v77\n\tv_exp_f32 v78, v78\n\tv_exp_f32 v79, v79\n\t" \
    "v_exp_f32 v80, v80\n\tv_exp_f32 v81, v81\n\tv_smfmac_f32_32x32x32_bf16 %[o0], %[v1a], v[72:79], %[idx]\n\tv_exp_f32 v82, v82\n\t" \
    "v_exp_f32 v83, v83\n\tv_exp_f32 v84, v84\n\tv_mfma_f32_32x32x16_bf16 v[64:79], %[ka], %[q2], %[ci]\n\tv_exp_f32 v85, v85\n\t" \
    "v_exp_f32 v86, v86\n\tv_exp_f32 v87, v87\n\tv_exp_f32 v88, v88\n\tv_exp_f32 v89, v89\n\t" \
    "v_smfmac_f32_32x32x32_bf16 %[o1], %[v0a], v[80:87], %[idx]\n\tv_exp_f32 v90, v90\n\tv_exp_f32 v91, v91\n\tv_exp_f32 v92, v92\n\t" \
    "v_exp_f32 v93, v93\n\tv_exp_f32 v94, v94\n\tv_exp_f32 v95, v95\n\tv_exp_f32 v64, v64\n\t" \
    "v_exp_f32 v65, v65\n\tv_smfmac_f32_32x32x32_bf16 %[o1], %[v1a], v[88:95], %[idx]\n\ts_waitcnt lgkmcnt(0)\n\tv_exp_f32 v66, v66\n\t" \
    "v_exp_f32 v67, v67\n\tv_exp_f32 v68, v68\n\tv_mfma_f32_32x32x16_bf16 v[80:95], %[ka], %[q3], %[ci]\n\tds_read_b128 %[ka], %[ad] offset:32768\n\t" \
    "v_exp_f32 v69, v69\n\tv_exp_f32 v70, v70\n\tv_exp_f32 v71, v71\n\tv_exp_f32 v72, v72\n\t" \
    "v_exp_f32 v73, v73\n\tv_smfmac_f32_32x32x32_bf16 %[o2], %[v0a], v[64:71], %[idx]\n\tv_exp_f32 v74, v74\n\tv_exp_f32 v75, v75\n\t" \
    "v_exp_f32 v76, v76\n\tv_exp_f32 v77, v77\n\tv_exp_f32 v78, v78\n\tv_exp_f32 v79, v79\n\t" \
    "v_exp_f32 v80, v80\n\tv_exp_f32 v81, v81\n\tv_smfmac_f32_32x32x32_bf16 %[o2], %[v1a], v[72:79], %[idx]\n\tv_exp_f32 v82, v82\n\t" \
    "v_exp_f32 v83, v83\n\tv_exp_f32 v84, v84\n\tv_mfma_f32_32x32x16_bf16 v[64:79], %[kb], %[q0], %[ci]\n\tv_exp_f32 v85, v85\n\t" \
    "v_exp_f32 v86, v86\n\tv_exp_f32 v87, v87\n\tv_exp_f32 v88, v88\n\tv_exp_f32 v89, v89\n\t" \
    "v_smfmac_f32_32x32x32_bf16 %[o3], %[v0a], v[80:87], %[idx]\n\tv_exp_f32 v90, v90\n\tv_exp_f32 v91, v91\n\tv_exp_f32 v92, v92\n\t" \
    "v_exp_f32 v93, v93\n\tv_exp_f32 v94, v94\n\tv_exp_f32 v95, v95\n\tv_exp_f32 v64, v64\n\t" \
    "v_exp_f32 v65, v65\n\tv_smfmac_f32_32x32x32_bf16 %[o3], %[v1a], v[88:95], %[idx]\n\ts_mov_b64 exec, %[msk]\n\tds_read_b128 %[v1a], %[ad] offset:34048\n\t" \
    "ds_read_b128 %[v0a], %[ad] offset:33792\n\ts_mov_b64 exec, -1\n\tv_exp_f32 v66, v66\n\tv_exp_f32 v67, v67\n\t" \
    "v_exp_f32 v68, v68\n\tv_mfma_f32_32x32x16_bf16 v[80:95], %[kb], %[q1], %[ci]\n\tv_exp_f32 v69, v69\n\tv_exp_f32 v70, v70\n\t" \
    "v_exp_f32 v71, v71\n\tv_exp_f32 v72, v72\n\tv_exp_f32 v73, v73\n\tv_smfmac_f32_32x32x32_bf16 %[o0], %[v0b], v[64:71], %[idx]\n\t" \
    "v_exp_f32 v74, v74\n\tv_exp_f32 v75, v75\n\tv_exp_f32 v76, v76\n\tv_exp_f32 v77, v77\n\t" \
    "v_exp_f32 v78, v78\n\tv_exp_f32 v79, v79\n\tv_exp_f32 v80, v80\n\tv_exp_f32 v81, v81\n\t" \
    "v_smfmac_f32_32x32x32_bf16 %[o0], %[v1b], v[72:79], %[idx]\n\tv_exp_f32 v82, v82\n\tv_exp_f32 v83, v83\n\tv_exp_f32 v84, v84\n\t" \
    "v_mfma_f32_32x32x16_bf16 v[64:79], %[kb], %[q2], %[ci]\n\tv_exp_f32 v85, v85\n\tv_exp_f32 v86, v86\n\tv_exp_f32 v87, v87\n\t" \
    "v_exp_f32 v88, v88\n\tv_exp_f32 v89, v89\n\tv_smfmac_f32_32x32x32_bf16 %[o1], %[v0b], v[80:87], %[idx]\n\tv_exp_f32 v90, v90\n\t" \
    "v_exp_f32 v91, v91\n\tv_exp_f32 v92, v92\n\tv_exp_f32 v93, v93\n\tv_exp_f32 v94, v94\n\t" \
    "v_exp_f32 v95, v95\n\tv_exp_f32 v64, v64\n\tv_exp_f32 v65, v65\n\tv_smfmac_f32_32x32x32_bf16 %[o1], %[v1b], v[88:95], %[idx]\n\t" \
    "s_waitcnt lgkmcnt(0)\n\tv_exp_f32 v66, v66\n\tv_exp_f32 v67, v67\n\tv_exp_f32 v68, v68\n\t" \
    "v_mfma_f32_32x32x16_bf16 v[80:95], %[kb], %[q3], %[ci]\n\tds_read_b128 %[kb], %[ad] offset:36864\n\tv_exp_f32 v69, v69\n\tv_exp_f32 v70, v70\n\t" \
    "v_exp_f32 v71, v71\n\tv_exp_f32 v72, v72\n\tv_exp_f32 v73, v73\n\tv_smfmac_f32_32x32x32_bf16 %[o2], %[v0b], v[64:71], %[idx]\n\t" \
    "v_exp_f32 v74, v74\n\tv_exp_f32 v75, v75\n\tv_exp_f32 v76, v76\n\tv_exp_f32 v77, v77\n\t" \
    "v_exp_f32 v78, v78\n\tv_exp_f32 v79, v79\n\tv_exp_f32 v80, v80\n\tv_exp_f32 v81, v81\n\t" \
    "v_smfmac_f32_32x32x32_bf16 %[o2], %[v1b], v[72:79], %[idx]\n\tv_exp_f32 v82, v82\n\tv_exp_f32 v83, v83\n\tv_exp_f32 v84, v84\n\t" \
    "v_mfma_f32_32x32x16_bf16 v[64:79], %[ka], %[q0], %[ci]\n\tv_exp_f32 v85, v85\n\tv_exp_f32 v86, v86\n\tv_exp_f32 v87, v87\n\t" \
    "v_exp_f32 v88, v88\n\tv_exp_f32 v89, v89\n\tv_smfmac_f32_32x32x32_bf16 %[o3], %[v0b], v[80:87], %[idx]\n\tv_add_u32 %[ad], 0x8000, %[ad]\n\t" \
    "v_exp_f32 v90, v90\n\tv_exp_f32 v91, v91\n\tv_exp_f32 v92, v92\n\tv_exp_f32 v93, v93\n\t" \
    "v_exp_f32 v94, v94\n\tv_exp_f32 v95, v95\n\tv_exp_f32 v64, v64\n\tv_exp_f32 v65, v65\n\t" \
    "v_smfmac_f32_32x32x32_bf16 %[o3], %[v1b], v[88:95], %[idx]\n\ts_mov_b64 exec, %[msk]\n\tds_read_b128 %[v1b], %[ad] offset:5376\n\tds_read_b128 %[v0b], %[ad] offset:5120\n\t" \
    "s_mov_b64 exec, -1\n\tv_exp_f32 v66, v66\n\tv_exp_f32 v67, v67\n\tv_exp_f32 v68, v68\n\t" \
    "v_mfma_f32_32x32x16_bf16 v[80:95], %[ka], %[q1], %[ci]\n\tv_exp_f32 v69, v69\n\tv_exp_f32 v70, v70\n\tv_exp_f32 v71, v71\n\t" \
    "v_exp_f32 v72, v72\n\tv_exp_f32 v73, v73\n\tv_smfmac_f32_32x32x32_bf16 %[o0], %[v0a], v[64:71], %[idx]\n\tv_exp_f32 v74, v74\n\t" \
    "v_exp_f32 v75, v75\n\tv_exp_f32 v76, v76\n\tv_exp_f32 v77, v77\n\tv_exp_f32 v78, v78\n\t" \
    "v_exp_f32 v79, v79\n\tv_exp_f32 v80, v80\n\tv_exp_f32 v81, v81\n\tv_smfmac_f32_32x32x32_bf16 %[o0], %[v1a], v[72:79], %[idx]\n\t" \
    "v_exp_f32 v82, v82\n\tv_exp_f32 v83, v83\n\tv_exp_f32 v84, v84\n\tv_mfma_f32_32x32x16_bf16 v[64:79], %[ka], %[q2], %[ci]\n\t" \
    "v_exp_f32 v85, v85\n\tv_exp_f32 v86, v86\n\tv_exp_f32 v87, v87\n\tv_exp_f32 v88, v88\n\t" \
    "v_exp_f32 v89, v89\n\tv_smfmac_f32_32x32x32_bf16 %[o1], %[v0a], v[80:87], %[idx]\n\tv_exp_f32 v90, v90\n\tv_exp_f32 v91, v91\n\t" \
    "v_exp_f32 v92, v92\n\tv_exp_f32 v93, v93\n\tv_exp_f32 v94, v94\n\tv_exp_f32 v95, v95\n\t" \
    "v_exp_f32 v64, v64\n\tv_exp_f32 v65, v65\n\tv_smfmac_f32_32x32x32_bf16 %[o1], %[v1a], v[88:95], %[idx]\n\ts_waitcnt lgkmcnt(0)\n\t" \
    "v_exp_f32 v66, v66\n\tv_exp_f32 v67, v67\n\tv_exp_f32 v68, v68\n\tv_mfma_f32_32x32x16_bf16 v[80:95], %[ka], %[q3], %[ci]\n\t" \
    "ds_read_b128 %[ka], %[ad] offset:8192\n\tv_exp_f32 v69, v69\n\tv_exp_f32 v70, v70\n\tv_exp_f32 v71, v71\n\t" \
    "v_exp_f32 v72, v72\n\tv_exp_f32 v73, v73\n\tv_smfmac_f32_32x32x32_bf16 %[o2], %[v0a], v[64:71], %[idx]\n\tv_exp_f32 v74, v74\n\t" \
    "v_exp_f32 v75, v75\n\tv_exp_f32 v76, v76\n\tv_exp_f32 v77, v77\n\tv_exp_f32 v78, v78\n\t" \
    "v_exp_f32 v79, v79\n\tv_exp_f32 v80, v80\n\tv_exp_f32 v81, v81\n\tv_smfmac_f32_32x32x32_bf16 %[o2], %[v1a], v[72:79], %[idx]\n\t" \
    "v_exp_f32 v82, v82\n\tv_exp_f32 v83, v83\n\tv_exp_f32 v84, v84\n\tv_mfma_f32_32x32x16_bf16 v[64:79], %[kb], %[q0], %[ci]\n\t" \
    "v_exp_f32 v85, v85\n\tv_exp_f32 v86, v86\n\tv_exp_f32 v87, v87\n\tv_exp_f32 v88, v88\n\t" \
    "v_exp_f32 v89, v89\n\tv_smfmac_f32_32x32x32_bf16 %[o3], %[v0a], v[80:87], %[idx]\n\tv_exp_f32 v90, v90\n\tv_exp_f32 v91, v91\n\t" \
    "v_exp_f32 v92, v92\n\tv_exp_f32 v93, v93\n\tv_exp_f32 v94, v94\n\tv_exp_f32 v95, v95\n\t" \
    "v_exp_f32 v64, v64\n\tv_exp_f32 v65, v65\n\tv_smfmac_f32_32x32x32_bf16 %[o3], %[v1a], v[88:95], %[idx]\n\ts_mov_b64 exec, %[msk]\n\t" \
    "ds_read_b128 %[v1a], %[ad] offset:9472\n\tds_read_b128 %[v0a], %[ad] offset:9216\n\ts_mov_b64 exec, -1\n\tv_exp_f32 v66, v66\n\t" \
    "v_exp_f32 v67, v67\n\tv_exp_f32 v68, v68\n\tv_mfma_f32_32x32x16_bf16 v[80:95], %[kb], %[q1], %[ci]\n\tv_exp_f32 v69, v69\n\t" \
    "v_exp_f32 v70, v70\n\tv_exp_f32 v71, v71\n\tv_exp_f32 v72, v72\n\tv_exp_f32 v73, v73\n\t" \
    "v_smfmac_f32_32x32x32_bf16 %[o0], %[v0b], v[64:71], %[idx]\n\tv_exp_f32 v74, v74\n\tv_exp_f32 v75, v75\n\tv_exp_f32 v76, v76\n\t" \
    "v_exp_f32 v77, v77\n\tv_exp_f32 v78, v78\n\tv_exp_f32 v79, v79\n\tv_exp_f32 v80, v80\n\t" \
    "v_exp_f32 v81, v81\n\tv_smfmac_f32_32x32x32_bf16 %[o0], %[v1b], v[72:79], %[idx]\n\tv_exp_f32 v82, v82\n\tv_exp_f32 v83, v83\n\t" \
    "v_exp_f32 v84, v84\n\tv_mfma_f32_32x32x16_bf16 v[64:79], %[kb], %[q2], %[ci]\n\tv_exp_f32 v85, v85\n\tv_exp_f32 v86, v86\n\t" \
    "v_exp_f32 v87, v87\n\tv_exp_f32 v88, v88\n\tv_exp_f32 v89, v89\n\tv_smfmac_f32_32x32x32_bf16 %[o1], %[v0b], v[80:87], %[idx]\n\t" \
    "v_exp_f32 v90, v90\n\tv_exp_f32 v91, v91\n\tv_exp_f32 v92, v92\n\tv_exp_f32 v93, v93\n\t" \
    "v_exp_f32 v94, v94\n\tv_exp_f32 v95, v95\n\tv_exp_f32 v64, v64\n\tv_exp_f32 v65, v65\n\t" \
    "v_smfmac_f32_32x32x32_bf16 %[o1], %[v1b], v[88:95], %[idx]\n\ts_waitcnt lgkmcnt(0)\n\tv_exp_f32 v66, v66\n\tv_exp_f32 v67, v67\n\t" \
    "v_exp_f32 v68, v68\n\tv_mfma_f32_32x32x16_bf16 v[80:95], %[kb], %[q3], %[ci]\n\tds_read_b128 %[kb], %[ad] offset:12288\n\tv_exp_f32 v69, v69\n\t" \
    "v_exp_f32 v70, v70\n\tv_exp_f32 v71, v71\n\tv_exp_f32 v72, v72\n\tv_exp_f32 v73, v73\n\t" \
    "v_smfmac_f32_32x32x32_bf16 %[o2], %[v0b], v[64:71], %[idx]\n\tv_exp_f32 v74, v74\n\tv_exp_f32 v75, v75\n\tv_exp_f32 v76, v76\n\t" \
    "v_exp_f32 v77, v77\n\tv_exp_f32 v78, v78\n\tv_exp_f32 v79, v79\n\tv_exp_f32 v80, v80\n\t" \
    "v_exp_f32 v81, v81\n\tv_smfmac_f32_32x32x32_bf16 %[o2], %[v1b], v[72:79], %[idx]\n\tv_exp_f32 v82, v82\n\tv_exp_f32 v83, v83\n\t" \
    "v_exp_f32 v84, v84\n\tv_mfma_f32_32x32x16_bf16 v[64:79], %[ka], %[q0], %[ci]\n\tv_exp_f32 v85, v85\n\tv_exp_f32 v86, v86\n\t" \
    "v_exp_f32 v87, v87\n\tv_exp_f32 v88, v88\n\tv_exp_f32 v89, v89\n\tv_smfmac_f32_32x32x32_bf16 %[o3], %[v0b], v[80:87], %[idx]\n\t" \
    "v_exp_f32 v90, v90\n\tv_exp_f32 v91, v91\n\tv_exp_f32 v92, v92\n\tv_exp_f32 v93, v93\n\t" \
    "v_exp_f32 v94, v94\n\tv_exp_f32 v95, v95\n\tv_exp_f32 v64, v64\n\tv_exp_f32 v65, v65\n\t" \
    "v_smfmac_f32_32x32x32_bf16 %[o3], %[v1b], v[88:95], %[idx]\n\ts_mov_b64 exec, %[msk]\n\tds_read_b128 %[v1b], %[ad] offset:13568\n\tds_read_b128 %[v0b], %[ad] offset:13312\n\t" \
    "s_mov_b64 exec, -1\n\tv_exp_f32 v66, v66\n\tv_exp_f32 v67, v67\n\tv_exp_f32 v68, v68\n\t" \
    "v_mfma_f32_32x32x16_bf16 v[80:95], %[ka], %[q1], %[ci]\n\tv_exp_f32 v69, v69\n\tv_exp_f32 v70, v70\n\tv_exp_f32 v71, v71\n\t" \
    "v_exp_f32 v72, v72\n\tv_exp_f32 v73, v73\n\tv_smfmac_f32_32x32x32_bf16 %[o0], %[v0a], v[64:71], %[idx]\n\tv_exp_f32 v74, v74\n\t" \
    "v_exp_f32 v75, v75\n\tv_exp_f32 v76, v76\n\tv_exp_f32 v77, v77\n\tv_exp_f32 v78, v78\n\t" \
    "v_exp_f32 v79, v79\n\tv_exp_f32 v80, v80\n\tv_exp_f32 v81, v81\n\tv_smfmac_f32_32x32x32_bf16 %[o0], %[v1a], v[72:79], %[idx]\n\t" \
    "v_exp_f32 v82, v82\n\tv_exp_f32 v83, v83\n\tv_exp_f32 v84, v84\n\tv_mfma_f32_32x32x16_bf16 v[64:79], %[ka], %[q2], %[ci]\n\t" \
    "v_exp_f32 v85, v85\n\tv_exp_f32 v86, v86\n\tv_exp_f32 v87, v87\n\tv_exp_f32 v88, v88\n\t" \
    "v_exp_f32 v89, v89\n\tv_smfmac_f32_32x32x32_bf16 %[o1], %[v0a], v[80:87], %[idx]\n\tv_exp_f32 v90, v90\n\tv_exp_f32 v91, v91\n\t" \
    "v_exp_f32 v92, v92\n\tv_exp_f32 v93, v93\n\tv_exp_f32 v94, v94\n\tv_exp_f32 v95, v95\n\t" \
    "v_exp_f32 v64, v64\n\tv_exp_f32 v65, v65\n\tv_smfmac_f32_32x32x32_bf16 %[o1], %[v1a], v[88:95], %[idx]\n\ts_waitcnt lgkmcnt(0)\n\t" \
    "v_exp_f32 v66, v66\n\tv_exp_f32 v67, v67\n\tv_exp_f32 v68, v68\n\tv_mfma_f32_32x32x16_bf16 v[80:95], %[ka], %[q3], %[ci]\n\t" \
    "ds_read_b128 %[ka], %[ad] offset:16384\n\tv_exp_f32 v69, v69\n\tv_exp_f32 v70, v70\n\tv_exp_f32 v71, v71\n\t" \
    "v_exp_f32 v72, v72\n\tv_exp_f32 v73, v73\n\tv_smfmac_f32_32x32x32_bf16 %[o2], %[v0a], v[64:71], %[idx]\n\tv_exp_f32 v74, v74\n\t" \
    "v_exp_f32 v75, v75\n\tv_exp_f32 v76, v76\n\tv_exp_f32 v77, v77\n\tv_exp_f32 v78, v78\n\t" \
    "v_exp_f32 v79, v79\n\tv_exp_f32 v80, v80\n\tv_exp_f32 v81, v81\n\tv_smfmac_f32_32x32x32_bf16 %[o2], %[v1a], v[72:79], %[idx]\n\t" \
    "v_exp_f32 v82, v82\n\tv_exp_f32 v83, v83\n\tv_exp_f32 v84, v84\n\tv_mfma_f32_32x32x16_bf16 v[64:79], %[kb], %[q0], %[ci]\n\t" \
    "v_exp_f32 v85, v85\n\tv_exp_f32 v86, v86\n\tv_exp_f32 v87, v87\n\tv_exp_f32 v88, v88\n\t" \
    "v_exp_f32 v89, v89\n\tv_smfmac_f32_32x32x32_bf16 %[o3], %[v0a], v[80:87], %[idx]\n\tv_exp_f32 v90, v90\n\tv_exp_f32 v91, v91\n\t" \
    "v_exp_f32 v92, v92\n\tv_exp_f32 v93, v93\n\tv_exp_f32 v94, v94\n\tv_exp_f32 v95, v95\n\t" \
    "v_exp_f32 v64, v64\n\tv_exp_f32 v65, v65\n\tv_smfmac_f32_32x32x32_bf16 %[o3], %[v1a], v[88:95], %[idx]\n\ts_mov_b64 exec, %[msk]\n\t" \
    "ds_read_b128 %[v1a], %[ad] offset:17664\n\tds_read_b128 %[v0a], %[ad] offset:17408\n\ts_mov_b64 exec, -1\n\tv_exp_f32 v66, v66\n\t" \
    "v_exp_f32 v67, v67\n\tv_exp_f32 v68, v68\n\tv_mfma_f32_32x32x16_bf16 v[80:95], %[kb], %[q1], %[ci]\n\tv_exp_f32 v69, v69\n\t" \
    "v_exp_f32 v70, v70\n\tv_exp_f32 v71, v71\n\tv_exp_f32 v72, v72\n\tv_exp_f32 v73, v73\n\t" \
    "v_smfmac_f32_32x32x32_bf16 %[o0], %[v0b], v[64:71], %[idx]\n\tv_exp_f32 v74, v74\n\tv_exp_f32 v75, v75\n\tv_exp_f32 v76, v76\n\t" \
    "v_exp_f32 v77, v77\n\tv_exp_f32 v78, v78\n\tv_exp_f32 v79, v79\n\tv_exp_f32 v80, v80\n\t" \
    "v_exp_f32 v81, v81\n\tv_smfmac_f32_32x32x32_bf16 %[o0], %[v1b], v[72:79], %[idx]\n\tv_exp_f32 v82, v82\n\tv_exp_f32 v83, v83\n\t" \
    "v_exp_f32 v84, v84\n\tv_mfma_f32_32x32x16_bf16 v[64:79], %[kb], %[q2], %[ci]\n\tv_exp_f32 v85, v85\n\tv_exp_f32 v86, v86\n\t" \
    "v_exp_f32 v87, v87\n\tv_exp_f32 v88, v88\n\tv_exp_f32 v89, v89\n\tv_smfmac_f32_32x32x32_bf16 %[o1], %[v0b], v[80:87], %[idx]\n\t" \
    "v_exp_f32 v90, v90\n\tv_exp_f32 v91, v91\n\tv_exp_f32 v92, v92\n\tv_exp_f32 v93, v93\n\t" \
    "v_exp_f32 v94, v94\n\tv_exp_f32 v95, v95\n\tv_exp_f32 v64, v64\n\tv_exp_f32 v65, v65\n\t" \
    "v_smfmac_f32_32x32x32_bf16 %[o1], %[v1b], v[88:95], %[idx]\n\ts_waitcnt lgkmcnt(0)\n\tv_exp_f32 v66, v66\n\tv_exp_f32 v67, v67\n\t" \
    "v_exp_f32 v68, v68\n\tv_mfma_f32_32x32x16_bf16 v[80:95], %[kb], %[q3], %[ci]\n\tds_read_b128 %[kb], %[ad] offset:20480\n\tv_exp_f32 v69, v69\n\t" \
    "v_exp_f32 v70, v70\n\tv_exp_f32 v71, v71\n\tv_exp_f32 v72, v72\n\tv_exp_f32 v73, v73\n\t" \
    "v_smfmac_f32_32x32x32_bf16 %[o2], %[v0b], v[64:71], %[idx]\n\tv_exp_f32 v74, v74\n\tv_exp_f32 v75, v75\n\tv_exp_f32 v76, v76\n\t" \
    "v_exp_f32 v77, v77\n\tv_exp_f32 v78, v78\n\tv_exp_f32 v79, v79\n\tv_exp_f32 v80, v80\n\t" \
    "v_exp_f32 v81, v81\n\tv_smfmac_f32_32x32x32_bf16 %[o2], %[v1b], v[72:79], %[idx]\n\tv_exp_f32 v82, v82\n\tv_exp_f32 v83, v83\n\t" \
    "v_exp_f32 v84, v84\n\tv_mfma_f32_32x32x16_bf16 v[64:79], %[ka], %[q0], %[ci]\n\tv_exp_f32 v85, v85\n\tv_exp_f32 v86, v86\n\t" \
    "v_exp_f32 v87, v87\n\tv_exp_f32 v88, v88\n\tv_exp_f32 v89, v89\n\tv_smfmac_f32_32x32x32_bf16 %[o3], %[v0b], v[80:87], %[idx]\n\t" \
    "v_exp_f32 v90, v90\n\tv_exp_f32 v91, v91\n\tv_exp_f32 v92, v92\n\tv_exp_f32 v93, v93\n\t" \
    "v_exp_f32 v94, v94\n\tv_exp_f32 v95, v95\n\tv_exp_f32 v64, v64\n\tv_exp_f32 v65, v65\n\t" \
    "v_smfmac_f32_32x32x32_bf16 %[o3], %[v1b], v[88:95], %[idx]\n\ts_mov_b64 exec, %[msk]\n\tds_read_b128 %[v1b], %[ad] offset:21760\n\tds_read_b128 %[v0b], %[ad] offset:21504\n\t" \
    "s_mov_b64 exec, -1\n\tv_exp_f32 v66, v66\n\tv_exp_f32 v67, v67\n\tv_exp_f32 v68, v68\n\t" \
    "v_mfma_f32_32x32x16_bf16 v[80:95], %[ka], %[q1], %[ci]\n\tv_exp_f32 v69, v69\n\tv_exp_f32 v70, v70\n\tv_exp_f32 v71, v71\n\t" \
    "v_exp_f32 v72, v72\n\tv_exp_f32 v73, v73\n\tv_smfmac_f32_32x32x32_bf16 %[o0], %[v0a], v[64:71], %[idx]\n\tv_exp_f32 v74, v74\n\t" \
    "v_exp_f32 v75, v75\n\tv_exp_f32 v76, v76\n\tv_exp_f32 v77, v77\n\tv_exp_f32 v78, v78\n\t" \
    "v_exp_f32 v79, v79\n\tv_exp_f32 v80, v80\n\tv_exp_f32 v81, v81\n\tv_smfmac_f32_32x32x32_bf16 %[o0], %[v1a], v[72:79], %[idx]\n\t" \
    "v_exp_f32 v82, v82\n\tv_exp_f32 v83, v83\n\tv_exp_f32 v84, v84\n\tv_mfma_f32_32x32x16_bf16 v[64:79], %[ka], %[q2], %[ci]\n\t" \
    "v_exp_f32 v85, v85\n\tv_exp_f32 v86, v86\n\tv_exp_f32 v87, v87\n\tv_exp_f32 v88, v88\n\t" \
    "v_exp_f32 v89, v89\n\tv_smfmac_f32_32x32x32_bf16 %[o1], %[v0a], v[80:87], %[idx]\n\tv_exp_f32 v90, v90\n\tv_exp_f32 v91, v91\n\t" \
    "v_exp_f32 v92, v92\n\tv_exp_f32 v93, v93\n\tv_exp_f32 v94, v94\n\tv_exp_f32 v95, v95\n\t" \
    "v_exp_f32 v64, v64\n\tv_exp_f32 v65, v65\n\tv_smfmac_f32_32x32x32_bf16 %[o1], %[v1a], v[88:95], %[idx]\n\ts_waitcnt lgkmcnt(0)\n\t" \
    "v_exp_f32 v66, v66\n\tv_exp_f32 v67, v67\n\tv_exp_f32 v68, v68\n\tv_mfma_f32_32x32x16_bf16 v[80:95], %[ka], %[q3], %[ci]\n\t" \
    "ds_read_b128 %[ka], %[ad] offset:24576\n\tv_exp_f32 v69, v69\n\tv_exp_f32 v70, v70\n\tv_exp_f32 v71, v71\n\t" \
    "v_exp_f32 v72, v72\n\tv_exp_f32 v73, v73\n\tv_smfmac_f32_32x32x32_bf16 %[o2], %[v0a], v[64:71], %[idx]\n\tv_exp_f32 v74, v74\n\t" \
    "v_exp_f32 v75, v75\n\tv_exp_f32 v76, v76\n\tv_exp_f32 v77, v77\n\tv_exp_f32 v78, v78\n\t" \
    "v_exp_f32 v79, v79\n\tv_exp_f32 v80, v80\n\tv_exp_f32 v81, v81\n\tv_smfmac_f32_32x32x32_bf16 %[o2], %[v1a], v[72:79], %[idx]\n\t" \
    "v_exp_f32 v82, v82\n\tv_exp_f32 v83, v83\n\tv_exp_f32 v84, v84\n\tv_mfma_f32_32x32x16_bf16 v[64:79], %[kb], %[q0], %[ci]\n\t" \
    "v_exp_f32 v85, v85\n\tv_exp_f32 v86, v86\n\tv_exp_f32 v87, v87\n\tv_exp_f32 v88, v88\n\t" \
    "v_exp_f32 v89, v89\n\tv_smfmac_f32_32x32x32_bf16 %[o3], %[v0a], v[80:87], %[idx]\n\tv_exp_f32 v90, v90\n\tv_exp_f32 v91, v91\n\t" \
    "v_exp_f32 v92, v92\n\tv_exp_f32 v93, v93\n\tv_exp_f32 v94, v94\n\tv_exp_f32 v95, v95\n\t" \
    "v_exp_f32 v64, v64\n\tv_exp_f32 v65, v65\n\tv_smfmac_f32_32x32x32_bf16 %[o3], %[v1a], v[88:95], %[idx]\n\ts_mov_b64 exec, %[msk]\n\t" \
    "ds_read_b128 %[v1a], %[ad] offset:25856\n\tds_read_b128 %[v0a], %[ad] offset:25600\n\ts_mov_b64 exec, -1\n\tv_exp_f32 v66, v66\n\t" \
    "v_exp_f32 v67, v67\n\tv_exp_f32 v68, v68\n\tv_mfma_f32_32x32x16_bf16 v[80:95], %[kb], %[q1], %[ci]\n\tv_exp_f32 v69, v69\n\t" \
    "v_exp_f32 v70, v70\n\tv_exp_f32 v71, v71\n\tv_exp_f32 v72, v72\n\tv_exp_f32 v73, v73\n\t" \
    "v_smfmac_f32_32x32x32_bf16 %[o0], %[v0b], v[64:71], %[idx]\n\tv_exp_f32 v74, v74\n\tv_exp_f32 v75, v75\n\tv_exp_f32 v76, v76\n\t" \
    "v_exp_f32 v77, v77\n\tv_exp_f32 v78, v78\n\tv_exp_f32 v79, v79\n\tv_exp_f32 v80, v80\n\t" \
    "v_exp_f32 v81, v81\n\tv_smfmac_f32_32x32x32_bf16 %[o0], %[v1b], v[72:79], %[idx]\n\tv_exp_f32 v82, v82\n\tv_exp_f32 v83, v83\n\t" \
    "v_exp_f32 v84, v84\n\tv_mfma_f32_32x32x16_bf16 v[64:79], %[kb], %[q2], %[ci]\n\tv_exp_f32 v85, v85\n\tv_exp_f32 v86, v86\n\t" \
    "v_exp_f32 v87, v87\n\tv_exp_f32 v88, v88\n\tv_exp_f32 v89, v89\n\tv_smfmac_f32_32x32x32_bf16 %[o1], %[v0b], v[80:87], %[idx]\n\t" \
    "v_exp_f32 v90, v90\n\tv_exp_f32 v91, v91\n\tv_exp_f32 v92, v92\n\tv_exp_f32 v93, v93\n\t" \
    "v_exp_f32 v94, v94\n\tv_exp_f32 v95, v95\n\tv_exp_f32 v64, v64\n\tv_exp_f32 v65, v65\n\t" \
    "v_smfmac_f32_32x32x32_bf16 %[o1], %[v1b], v[88:95], %[idx]\n\ts_waitcnt lgkmcnt(0)\n\tv_exp_f32 v66, v66\n\tv_exp_f32 v67, v67\n\t" \
    "v_exp_f32 v68, v68\n\tv_mfma_f32_32x32x16_bf16 v[80:95], %[kb], %[q3], %[ci]\n\tds_read_b128 %[kb], %[ad] offset:28672\n\tv_exp_f32 v69, v69\n\t" \
    "v_exp_f32 v70, v70\n\tv_exp_f32 v71, v71\n\tv_exp_f32 v72, v72\n\tv_exp_f32 v73, v73\n\t" \
    "v_smfmac_f32_32x32x32_bf16 %[o2], %[v0b], v[64:71], %[idx]\n\tv_exp_f32 v74, v74\n\tv_exp_f32 v75, v75\n\tv_exp_f32 v76, v76\n\t" \
    "v_exp_f32 v77, v77\n\tv_exp_f32 v78, v78\n\tv_exp_f32 v79, v79\n\tv_exp_f32 v80, v80\n\t" \
    "v_exp_f32 v81, v81\n\tv_smfmac_f32_32x32x32_bf16 %[o2], %[v1b], v[72:79], %[idx]\n\tv_exp_f32 v82, v82\n\tv_exp_f32 v83, v83\n\t" \
    "v_exp_f32 v84, v84\n\tv_mfma_f32_32x32x16_bf16 v[64:79], %[ka], %[q0], %[ci]\n\tv_exp_f32 v85, v85\n\tv_exp_f32 v86, v86\n\t" \
    "v_exp_f32 v87, v87\n\tv_exp_f32 v88, v88\n\tv_exp_f32 v89, v89\n\tv_smfmac_f32_32x32x32_bf16 %[o3], %[v0b], v[80:87], %[idx]\n\t" \
    "v_exp_f32 v90, v90\n\tv_exp_f32 v91, v91\n\tv_exp_f32 v92, v92\n\tv_exp_f32 v93, v93\n\t" \
    "v_exp_f32 v94, v94\n\tv_exp_f32 v95, v95\n\tv_exp_f32 v64, v64\n\tv_exp_f32 v65, v65\n\t" \
    "v_smfmac_f32_32x32x32_bf16 %[o3], %[v1b], v[88:95], %[idx]\n\ts_mov_b64 exec, %[msk]\n\tds_read_b128 %[v1b], %[ad] offset:29952\n\tds_read_b128 %[v0b], %[ad] offset:29696\n\t" \
    "s_mov_b64 exec, -1\n\tv_exp_f32 v66, v66\n\tv_exp_f32 v67, v67\n\tv_exp_f32 v68, v68\n\t" \
    "v_mfma_f32_32x32x16_bf16 v[80:95], %[ka], %[q1], %[ci]\n\tv_exp_f32 v69, v69\n\tv_exp_f32 v70, v70\n\tv_exp_f32 v71, v71\n\t" \
    "v_exp_f32 v72, v72\n\tv_exp_f32 v73, v73\n\tv_smfmac_f32_32x32x32_bf16 %[o0], %[v0a], v[64:71], %[idx]\n\tv_exp_f32 v74, v74\n\t" \
    "v_exp_f32 v75, v75\n\tv_exp_f32 v76, v76\n\tv_exp_f32 v77, v77\n\tv_exp_f32 v78, v78\n\t" \
    "v_exp_f32 v79, v79\n\tv_exp_f32 v80, v80\n\tv_exp_f32 v81, v81\n\tv_smfmac_f32_32x32x32_bf16 %[o0], %[v1a], v[72:79], %[idx]\n\t" \
    "v_exp_f32 v82, v82\n\tv_exp_f32 v83, v83\n\tv_exp_f32 v84, v84\n\tv_mfma_f32_32x32x16_bf16 v[64:79], %[ka], %[q2], %[ci]\n\t" \
    "v_exp_f32 v85, v85\n\tv_exp_f32 v86, v86\n\tv_exp_f32 v87, v87\n\tv_exp_f32 v88, v88\n\t" \
    "v_exp_f32 v89, v89\n\tv_smfmac_f32_32x32x32_bf16 %[o1], %[v0a], v[80:87], %[idx]\n\tv_exp_f32 v90, v90\n\tv_exp_f32 v91, v91\n\t" \
    "v_exp_f32 v92, v92\n\tv_exp_f32 v93, v93\n\tv_exp_f32 v94, v94\n\tv_exp_f32 v95, v95\n\t" \
    "v_exp_f32 v64, v64\n\tv_exp_f32 v65, v65\n\tv_smfmac_f32_32x32x32_bf16 %[o1], %[v1a], v[88:95], %[idx]\n\ts_waitcnt lgkmcnt(0)\n\t" \
    "v_exp_f32 v66, v66\n\tv_exp_f32 v67, v67\n\tv_exp_f32 v68, v68\n\tv_mfma_f32_32x32x16_bf16 v[80:95], %[ka], %[q3], %[ci]\n\t" \
    "ds_read_b128 %[ka], %[ad] offset:32768\n\tv_exp_f32 v69, v69\n\tv_exp_f32 v70, v70\n\tv_exp_f32 v71, v71\n\t" \
    "v_exp_f32 v72, v72\n\tv_exp_f32 v73, v73\n\tv_smfmac_f32_32x32x32_bf16 %[o2], %[v0a], v[64:71], %[idx]\n\tv_exp_f32 v74, v74\n\t" \
    "v_exp_f32 v75, v75\n\tv_exp_f32 v76, v76\n\tv_exp_f32 v77, v77\n\tv_exp_f32 v78, v78\n\t" \
    "v_exp_f32 v79, v79\n\tv_exp_f32 v80, v80\n\tv_exp_f32 v81, v81\n\tv_smfmac_f32_32x32x32_bf16 %[o2], %[v1a], v[72:79], %[idx]\n\t" \
    "v_exp_f32 v82, v82\n\tv_exp_f32 v83, v83\n\tv_exp_f32 v84, v84\n\tv_mfma_f32_32x32x16_bf16 v[64:79], %[kb], %[q0], %[ci]\n\t" \
    "v_exp_f32 v85, v85\n\tv_exp_f32 v86, v86\n\tv_exp_f32 v87, v87\n\tv_exp_f32 v88, v88\n\t" \
    "v_exp_f32 v89, v89\n\tv_smfmac_f32_32x32x32_bf16 %[o3], %[v0a], v[80:87], %[idx]\n\tv_exp_f32 v90, v90\n\tv_exp_f32 v91, v91\n\t" \
    "v_exp_f32 v92, v92\n\tv_exp_f32 v93, v93\n\tv_exp_f32 v94, v94\n\tv_exp_f32 v95, v95\n\t" \
    "v_exp_f32 v64, v64\n\tv_exp_f32 v65, v65\n\tv_smfmac_f32_32x32x32_bf16 %[o3], %[v1a], v[88:95], %[idx]\n\ts_mov_b64 exec, %[msk]\n\t" \
    "ds_read_b128 %[v1a], %[ad] offset:34048\n\tds_read_b128 %[v0a], %[ad] offset:33792\n\ts_mov_b64 exec, -1\n\tv_exp_f32 v66, v66\n\t" \
    "v_exp_f32 v67, v67\n\tv_exp_f32 v68, v68\n\tv_mfma_f32_32x32x16_bf16 v[80:95], %[kb], %[q1], %[ci]\n\tv_exp_f32 v69, v69\n\t" \
    "v_exp_f32 v70, v70\n\tv_exp_f32 v71, v71\n\tv_exp_f32 v72, v72\n\tv_exp_f32 v73, v73\n\t" \
    "v_smfmac_f32_32x32x32_bf16 %[o0], %[v0b], v[64:71], %[idx]\n\tv_exp_f32 v74, v74\n\tv_exp_f32 v75, v75\n\tv_exp_f32 v76, v76\n\t" \
    "v_exp_f32 v77, v77\n\tv_exp_f32 v78, v78\n\tv_exp_f32 v79, v79\n\tv_exp_f32 v80, v80\n\t" \
    "v_exp_f32 v81, v81\n\tv_smfmac_f32_32x32x32_bf16 %[o0], %[v1b], v[72:79], %[idx]\n\tv_exp_f32 v82, v82\n\tv_exp_f32 v83, v83\n\t" \
    "v_exp_f32 v84, v84\n\tv_mfma_f32_32x32x16_bf16 v[64:79], %[kb], %[q2], %[ci]\n\tv_exp_f32 v85, v85\n\tv_exp_f32 v86, v86\n\t" \
    "v_exp_f32 v87, v87\n\tv_exp_f32 v88, v88\n\tv_exp_f32 v89, v89\n\tv_smfmac_f32_32x32x32_bf16 %[o1], %[v0b], v[80:87], %[idx]\n\t" \
    "v_exp_f32 v90, v90\n\tv_exp_f32 v91, v91\n\tv_exp_f32 v92, v92\n\tv_exp_f32 v93, v93\n\t" \
    "v_exp_f32 v94, v94\n\tv_exp_f32 v95, v95\n\tv_exp_f32 v64, v64\n\tv_exp_f32 v65, v65\n\t" \
    "v_smfmac_f32_32x32x32_bf16 %[o1], %[v1b], v[88:95], %[idx]\n\ts_waitcnt lgkmcnt(0)\n\tv_exp_f32 v66, v66\n\tv_exp_f32 v67, v67\n\t" \
    "v_exp_f32 v68, v68\n\tv_mfma_f32_32x32x16_bf16 v[80:95], %[kb], %[q3], %[ci]\n\tds_read_b128 %[kb], %[ad] offset:36864\n\tv_exp_f32 v69, v69\n\t" \
    "v_exp_f32 v70, v70\n\tv_exp_f32 v71, v71\n\tv_exp_f32 v72, v72\n\tv_exp_f32 v73, v73\n\t" \
    "v_smfmac_f32_32x32x32_bf16 %[o2], %[v0b], v[64:71], %[idx]\n\tv_exp_f32 v74, v74\n\tv_exp_f32 v75, v75\n\tv_exp_f32 v76, v76\n\t" \
    "v_exp_f32 v77, v77\n\tv_exp_f32 v78, v78\n\tv_exp_f32 v79, v79\n\tv_exp_f32 v80, v80\n\t" \
    "v_exp_f32 v81, v81\n\tv_smfmac_f32_32x32x32_bf16 %[o2], %[v1b], v[72:79], %[idx]\n\tv_exp_f32 v82, v82\n\tv_exp_f32 v83, v83\n\t" \
    "v_exp_f32 v84, v84\n\tv_mfma_f32_32x32x16_bf16 v[64:79], %[ka], %[q0], %[ci]\n\tv_exp_f32 v85, v85\n\tv_exp_f32 v86, v86\n\t" \
    "v_exp_f32 v87, v87\n\tv_exp_f32 v88, v88\n\tv_exp_f32 v89, v89\n\tv_smfmac_f32_32x32x32_bf16 %[o3], %[v0b], v[80:87], %[idx]\n\t" \
    "s_barrier\n\tv_add_u32 %[ad], 0x8000, %[ad]\n\tv_exp_f32 v90, v90\n\tv_exp_f32 v91, v91\n\t" \
    "v_exp_f32 v92, v92\n\tv_exp_f32 v93, v93\n\tv_exp_f32 v94, v94\n\tv_exp_f32 v95, v95\n\t" \
    "v_exp_f32 v64, v64\n\tv_exp_f32 v65, v65\n\tv_smfmac_f32_32x32x32_bf16 %[o3], %[v1b], v[88:95], %[idx]\n\ts_mov_b64 exec, %[msk]\n\t" \
    "ds_read_b128 %[v1b], %[ad] offset:5376\n\tds_read_b128 %[v0b], %[ad] offset:5120\n\ts_mov_b64 exec, -1\n\tv_exp_f32 v66, v66\n\t" \
    "v_exp_f32 v67, v67\n\tv_exp_f32 v68, v68\n\tv_mfma_f32_32x32x16_bf16 v[80:95], %[ka], %[q1], %[ci]\n\tv_exp_f32 v69, v69\n\t" \
    "v_exp_f32 v70, v70\n\tv_exp_f32 v71, v71\n\tv_exp_f32 v72, v72\n\tv_exp_f32 v73, v73\n\t" \
    "v_smfmac_f32_32x32x32_bf16 %[o0], %[v0a], v[64:71], %[idx]\n\tv_exp_f32 v74, v74\n\tv_exp_f32 v75, v75\n\tv_exp_f32 v76, v76\n\t" \
    "v_exp_f32 v77, v77\n\tv_exp_f32 v78, v78\n\tv_exp_f32 v79, v79\n\tv_exp_f32 v80, v80\n\t" \
    "v_exp_f32 v81, v81\n\tv_smfmac_f32_32x32x32_bf16 %[o0], %[v1a], v[72:79], %[idx]\n\tv_exp_f32 v82, v82\n\tv_exp_f32 v83, v83\n\t" \
    "v_exp_f32 v84, v84\n\tv_mfma_f32_32x32x16_bf16 v[64:79], %[ka], %[q2], %[ci]\n\tv_exp_f32 v85, v85\n\tv_exp_f32 v86, v86\n\t" \
    "v_exp_f32 v87, v87\n\tv_exp_f32 v88, v88\n\tv_exp_f32 v89, v89\n\tv_smfmac_f32_32x32x32_bf16 %[o1], %[v0a], v[80:87], %[idx]\n\t" \
    "v_exp_f32 v90, v90\n\tv_exp_f32 v91, v91\n\tv_exp_f32 v92, v92\n\tv_exp_f32 v93, v93\n\t" \
    "v_exp_f32 v94, v94\n\tv_exp_f32 v95, v95\n\tv_exp_f32 v64, v64\n\tv_exp_f32 v65, v65\n\t" \
    "v_smfmac_f32_32x32x32_bf16 %[o1], %[v1a], v[88:95], %[idx]\n\ts_waitcnt lgkmcnt(0)\n\tv_exp_f32 v66, v66\n\tv_exp_f32 v67, v67\n\t" \
    "v_exp_f32 v68, v68\n\tv_mfma_f32_32x32x16_bf16 v[80:95], %[ka], %[q3], %[ci]\n\tds_read_b128 %[ka], %[ad] offset:8192\n\tv_exp_f32 v69, v69\n\t" \
    "v_exp_f32 v70, v70\n\tv_exp_f32 v71, v71\n\tv_exp_f32 v72, v72\n\tv_exp_f32 v73, v73\n\t" \
    "v_smfmac_f32_32x32x32_bf16 %[o2], %[v0a], v[64:71], %[idx]\n\tv_exp_f32 v74, v74\n\tv_exp_f32 v75, v75\n\tv_exp_f32 v76, v76\n\t" \
    "v_exp_f32 v77, v77\n\tv_exp_f32 v78, v78\n\tv_exp_f32 v79, v79\n\tv_exp_f32 v80, v80\n\t" \
    "v_exp_f32 v81, v81\n\tv_smfmac_f32_32x32x32_bf16 %[o2], %[v1a], v[72:79], %[idx]\n\tv_exp_f32 v82, v82\n\tv_exp_f32 v83, v83\n\t" \
    "v_exp_f32 v84, v84\n\tv_mfma_f32_32x32x16_bf16 v[64:79], %[kb], %[q0], %[ci]\n\tv_exp_f32 v85, v85\n\tv_exp_f32 v86, v86\n\t" \
    "v_exp_f32 v87, v87\n\tv_exp_f32 v88, v88\n\tv_exp_f32 v89, v89\n\tv_smfmac_f32_32x32x32_bf16 %[o3], %[v0a], v[80:87], %[idx]\n\t" \
    "v_exp_f32 v90, v90\n\tv_exp_f32 v91, v91\n\tv_exp_f32 v92, v92\n\tv_exp_f32 v93, v93\n\t" \
    "v_exp_f32 v94, v94\n\tv_exp_f32 v95, v95\n\tv_exp_f32 v64, v64\n\tv_exp_f32 v65, v65\n\t" \
    "v_smfmac_f32_32x32x32_bf16 %[o3], %[v1a], v[88:95], %[idx]\n\ts_mov_b64 exec, %[msk]\n\tds_read_b128 %[v1a], %[ad] offset:9472\n\tds_read_b128 %[v0a], %[ad] offset:9216\n\t" \
    "s_mov_b64 exec, -1\n\tv_exp_f32 v66, v66\n\tv_exp_f32 v67, v67\n\tv_exp_f32 v68, v68\n\t" \
    "v_mfma_f32_32x32x16_bf16 v[80:95], %[kb], %[q1], %[ci]\n\tv_exp_f32 v69, v69\n\tv_exp_f32 v70, v70\n\tv_exp_f32 v71, v71\n\t" \
    "v_exp_f32 v72, v72\n\tv_exp_f32 v73, v73\n\tv_smfmac_f32_32x32x32_bf16 %[o0], %[v0b], v[64:71], %[idx]\n\tv_exp_f32 v74, v74\n\t" \
    "v_exp_f32 v75, v75\n\tv_exp_f32 v76, v76\n\tv_exp_f32 v77, v77\n\tv_exp_f32 v78, v78\n\t" \
    "v_exp_f32 v79, v79\n\tv_exp_f32 v80, v80\n\tv_exp_f32 v81, v81\n\tv_smfmac_f32_32x32x32_bf16 %[o0], %[v1b], v[72:79], %[idx]\n\t" \
    "v_exp_f32 v82, v82\n\tv_exp_f32 v83, v83\n\tv_exp_f32 v84, v84\n\tv_mfma_f32_32x32x16_bf16 v[64:79], %[kb], %[q2], %[ci]\n\t" \
    "v_exp_f32 v85, v85\n\tv_exp_f32 v86, v86\n\tv_exp_f32 v87, v87\n\tv_exp_f32 v88, v88\n\t" \
    "v_exp_f32 v89, v89\n\tv_smfmac_f32_32x32x32_bf16 %[o1], %[v0b], v[80:87], %[idx]\n\tv_exp_f32 v90, v90\n\tv_exp_f32 v91, v91\n\t" \
    "v_exp_f32 v92, v92\n\tv_exp_f32 v93, v93\n\tv_exp_f32 v94, v94\n\tv_exp_f32 v95, v95\n\t" \
    "v_exp_f32 v64, v64\n\tv_exp_f32 v65, v65\n\tv_smfmac_f32_32x32x32_bf16 %[o1], %[v1b], v[88:95], %[idx]\n\ts_waitcnt lgkmcnt(0)\n\t" \
    "v_exp_f32 v66, v66\n\tv_exp_f32 v67, v67\n\tv_exp_f32 v68, v68\n\tv_mfma_f32_32x32x16_bf16 v[80:95], %[kb], %[q3], %[ci]\n\t" \
    "ds_read_b128 %[kb], %[ad] offset:12288\n\tv_exp_f32 v69, v69\n\tv_exp_f32 v70, v70\n\tv_exp_f32 v71, v71\n\t" \
    "v_exp_f32 v72, v72\n\tv_exp_f32 v73, v73\n\tv_smfmac_f32_32x32x32_bf16 %[o2], %[v0b], v[64:71], %[idx]\n\tv_exp_f32 v74, v74\n\t" \
    "v_exp_f32 v75, v75\n\tv_exp_f32 v76, v76\n\tv_exp_f32 v77, v77\n\tv_exp_f32 v78, v78\n\t" \
    "v_exp_f32 v79, v79\n\tv_exp_f32 v80, v80\n\tv_exp_f32 v81, v81\n\tv_smfmac_f32_32x32x32_bf16 %[o2], %[v1b], v[72:79], %[idx]\n\t" \
    "v_exp_f32 v82, v82\n\tv_exp_f32 v83, v83\n\tv_exp_f32 v84, v84\n\tv_mfma_f32_32x32x16_bf16 v[64:79], %[ka], %[q0], %[ci]\n\t" \
    "v_exp_f32 v85, v85\n\tv_exp_f32 v86, v86\n\tv_exp_f32 v87, v87\n\tv_exp_f32 v88, v88\n\t" \
    "v_exp_f32 v89, v89\n\tv_smfmac_f32_32x32x32_bf16 %[o3], %[v0b], v[80:87], %[idx]\n\tv_exp_f32 v90, v90\n\tv_exp_f32 v91, v91\n\t" \
    "v_exp_f32 v92, v92\n\tv_exp_f32 v93, v93\n\tv_exp_f32 v94, v94\n\tv_exp_f32 v95, v95\n\t" \
    "v_exp_f32 v64, v64\n\tv_exp_f32 v65, v65\n\tv_smfmac_f32_32x32x32_bf16 %[o3], %[v1b], v[88:95], %[idx]\n\ts_mov_b64 exec, %[msk]\n\t" \
    "ds_read_b128 %[v1b], %[ad] offset:13568\n\tds_read_b128 %[v0b], %[ad] offset:13312\n\ts_mov_b64 exec, -1\n\tv_exp_f32 v66, v66\n\t" \
    "v_exp_f32 v67, v67\n\tv_exp_f32 v68, v68\n\tv_mfma_f32_32x32x16_bf16 v[80:95], %[ka], %[q1], %[ci]\n\tv_exp_f32 v69, v69\n\t" \
    "v_exp_f32 v70, v70\n\tv_exp_f32 v71, v71\n\tv_exp_f32 v72, v72\n\tv_exp_f32 v73, v73\n\t" \
    "v_smfmac_f32_32x32x32_bf16 %[o0], %[v0a], v[64:71], %[idx]\n\tv_exp_f32 v74, v74\n\tv_exp_f32 v75, v75\n\tv_exp_f32 v76, v76\n\t" \
    "v_exp_f32 v77, v77\n\tv_exp_f32 v78, v78\n\tv_exp_f32 v79, v79\n\tv_exp_f32 v80, v80\n\t" \
    "v_exp_f32 v81, v81\n\tv_smfmac_f32_32x32x32_bf16 %[o0], %[v1a], v[72:79], %[idx]\n\tv_exp_f32 v82, v82\n\tv_exp_f32 v83, v83\n\t" \
    "v_exp_f32 v84, v84\n\tv_mfma_f32_32x32x16_bf16 v[64:79], %[ka], %[q2], %[ci]\n\tv_exp_f32 v85, v85\n\tv_exp_f32 v86, v86\n\t" \
    "v_exp_f32 v87, v87\n\tv_exp_f32 v88, v88\n\tv_exp_f32 v89, v89\n\tv_smfmac_f32_32x32x32_bf16 %[o1], %[v0a], v[80:87], %[idx]\n\t" \
    "v_exp_f32 v90, v90\n\tv_exp_f32 v91, v91\n\tv_exp_f32 v92, v92\n\tv_exp_f32 v93, v93\n\t" \
    "v_exp_f32 v94, v94\n\tv_exp_f32 v95, v95\n\tv_exp_f32 v64, v64\n\tv_exp_f32 v65, v65\n\t" \
    "v_smfmac_f32_32x32x32_bf16 %[o1], %[v1a], v[88:95], %[idx]\n\ts_waitcnt lgkmcnt(0)\n\tv_exp_f32 v66, v66\n\tv_exp_f32 v67, v67\n\t" \
    "v_exp_f32 v68, v68\n\tv_mfma_f32_32x32x16_bf16 v[80:95], %[ka], %[q3], %[ci]\n\tds_read_b128 %[ka], %[ad] offset:16384\n\tv_exp_f32 v69, v69\n\t" \
    "v_exp_f32 v70, v70\n\tv_exp_f32 v71, v71\n\tv_exp_f32 v72, v72\n\tv_exp_f32 v73, v73\n\t" \
    "v_smfmac_f32_32x32x32_bf16 %[o2], %[v0a], v[64:71], %[idx]\n\tv_exp_f32 v74, v74\n\tv_exp_f32 v75, v75\n\tv_exp_f32 v76, v76\n\t" \
    "v_exp_f32 v77, v77\n\tv_exp_f32 v78, v78\n\tv_exp_f32 v79, v79\n\tv_exp_f32 v80, v80\n\t" \
    "v_exp_f32 v81, v81\n\tv_smfmac_f32_32x32x32_bf16 %[o2], %[v1a], v[72:79], %[idx]\n\tv_exp_f32 v82, v82\n\tv_exp_f32 v83, v83\n\t" \
    "v_exp_f32 v84, v84\n\tv_mfma_f32_32x32x16_bf16 v[64:79], %[kb], %[q0], %[ci]\n\tv_exp_f32 v85, v85\n\tv_exp_f32 v86, v86\n\t" \
    "v_exp_f32 v87, v87\n\tv_exp_f32 v88, v88\n\tv_exp_f32 v89, v89\n\tv_smfmac_f32_32x32x32_bf16 %[o3], %[v0a], v[80:87], %[idx]\n\t" \
    "v_exp_f32 v90, v90\n\tv_exp_f32 v91, v91\n\tv_exp_f32 v92, v92\n\tv_exp_f32 v93, v93\n\t" \
    "v_exp_f32 v94, v94\n\tv_exp_f32 v95, v95\n\tv_exp_f32 v64, v64\n\tv_exp_f32 v65, v65\n\t" \
    "v_smfmac_f32_32x32x32_bf16 %[o3], %[v1a], v[88:95], %[idx]\n\ts_mov_b64 exec, %[msk]\n\tds_read_b128 %[v1a], %[ad] offset:17664\n\tds_read_b128 %[v0a], %[ad] offset:17408\n\t" \
    "s_mov_b64 exec, -1\n\tv_exp_f32 v66, v66\n\tv_exp_f32 v67, v67\n\tv_exp_f32 v68, v68\n\t" \
    "v_mfma_f32_32x32x16_bf16 v[80:95], %[kb], %[q1], %[ci]\n\tv_exp_f32 v69, v69\n\tv_exp_f32 v70, v70\n\tv_exp_f32 v71, v71\n\t" \
    "v_exp_f32 v72, v72\n\tv_exp_f32 v73, v73\n\tv_smfmac_f32_32x32x32_bf16 %[o0], %[v0b], v[64:71], %[idx]\n\tv_exp_f32 v74, v74\n\t" \
    "v_exp_f32 v75, v75\n\tv_exp_f32 v76, v76\n\tv_exp_f32 v77, v77\n\tv_exp_f32 v78, v78\n\t" \
    "v_exp_f32 v79, v79\n\tv_exp_f32 v80, v80\n\tv_exp_f32 v81, v81\n\tv_smfmac_f32_32x32x32_bf16 %[o0], %[v1b], v[72:79], %[idx]\n\t" \
    "v_exp_f32 v82, v82\n\tv_exp_f32 v83, v83\n\tv_exp_f32 v84, v84\n\tv_mfma_f32_32x32x16_bf16 v[64:79], %[kb], %[q2], %[ci]\n\t" \
    "v_exp_f32 v85, v85\n\tv_exp_f32 v86, v86\n\tv_exp_f32 v87, v87\n\tv_exp_f32 v88, v88\n\t" \
    "v_exp_f32 v89, v89\n\tv_smfmac_f32_32x32x32_bf16 %[o1], %[v0b], v[80:87], %[idx]\n\tv_exp_f32 v90, v90\n\tv_exp_f32 v91, v91\n\t" \
    "v_exp_f32 v92, v92\n\tv_exp_f32 v93, v93\n\tv_exp_f32 v94, v94\n\tv_exp_f32 v95, v95\n\t" \
    "v_exp_f32 v64, v64\n\tv_exp_f32 v65, v65\n\tv_smfmac_f32_32x32x32_bf16 %[o1], %[v1b], v[88:95], %[idx]\n\ts_waitcnt lgkmcnt(0)\n\t" \
    "v_exp_f32 v66, v66\n\tv_exp_f32 v67, v67\n\tv_exp_f32 v68, v68\n\tv_mfma_f32_32x32x16_bf16 v[80:95], %[kb], %[q3], %[ci]\n\t" \
    "ds_read_b128 %[kb], %[ad] offset:20480\n\tv_exp_f32 v69, v69\n\tv_exp_f32 v70, v70\n\tv_exp_f32 v71, v71\n\t" \
    "v_exp_f32 v72, v72\n\tv_exp_f32 v73, v73\n\tv_smfmac_f32_32x32x32_bf16 %[o2], %[v0b], v[64:71], %[idx]\n\tv_exp_f32 v74, v74\n\t" \
    "v_exp_f32 v75, v75\n\tv_exp_f32 v76, v76\n\tv_exp_f32 v77, v77\n\tv_exp_f32 v78, v78\n\t" \
    "v_exp_f32 v79, v79\n\tv_exp_f32 v80, v80\n\tv_exp_f32 v81, v81\n\tv_smfmac_f32_32x32x32_bf16 %[o2], %[v1b], v[72:79], %[idx]\n\t" \
    "v_exp_f32 v82, v82\n\tv_exp_f32 v83, v83\n\tv_exp_f32 v84, v84\n\tv_mfma_f32_32x32x16_bf16 v[64:79], %[ka], %[q0], %[ci]\n\t" \
    "v_exp_f32 v85, v85\n\tv_exp_f32 v86, v86\n\tv_exp_f32 v87, v87\n\tv_exp_f32 v88, v88\n\t" \
    "v_exp_f32 v89, v89\n\tv_smfmac_f32_32x32x32_bf16 %[o3], %[v0b], v[80:87], %[idx]\n\tv_exp_f32 v90, v90\n\tv_exp_f32 v91, v91\n\t" \
    "v_exp_f32 v92, v92\n\tv_exp_f32 v93, v93\n\tv_exp_f32 v94, v94\n\tv_exp_f32 v95, v95\n\t" \
    "v_exp_f32 v64, v64\n\tv_exp_f32 v65, v65\n\tv_smfmac_f32_32x32x32_bf16 %[o3], %[v1b], v[88:95], %[idx]\n\ts_mov_b64 exec, %[msk]\n\t" \
    "ds_read_b128 %[v1b], %[ad] offset:21760\n\tds_read_b128 %[v0b], %[ad] offset:21504\n\ts_mov_b64 exec, -1\n\tv_exp_f32 v66, v66\n\t" \
    "v_exp_f32 v67, v67\n\tv_exp_f32 v68, v68\n\tv_mfma_f32_32x32x16_bf16 v[80:95], %[ka], %[q1], %[ci]\n\tv_exp_f32 v69, v69\n\t" \
    "v_exp_f32 v70, v70\n\tv_exp_f32 v71, v71\n\tv_exp_f32 v72, v72\n\tv_exp_f32 v73, v73\n\t" \
    "v_smfmac_f32_32x32x32_bf16 %[o0], %[v0a], v[64:71], %[idx]\n\tv_exp_f32 v74, v74\n\tv_exp_f32 v75, v75\n\tv_exp_f32 v76, v76\n\t" \
    "v_exp_f32 v77, v77\n\tv_exp_f32 v78, v78\n\tv_exp_f32 v79, v79\n\tv_exp_f32 v80, v80\n\t" \
    "v_exp_f32 v81, v81\n\tv_smfmac_f32_32x32x32_bf16 %[o0], %[v1a], v[72:79], %[idx]\n\tv_exp_f32 v82, v82\n\tv_exp_f32 v83, v83\n\t" \
    "v_exp_f32 v84, v84\n\tv_mfma_f32_32x32x16_bf16 v[64:79], %[ka], %[q2], %[ci]\n\tv_exp_f32 v85, v85\n\tv_exp_f32 v86, v86\n\t" \
    "v_exp_f32 v87, v87\n\tv_exp_f32 v88, v88\n\tv_exp_f32 v89, v89\n\tv_smfmac_f32_32x32x32_bf16 %[o1], %[v0a], v[80:87], %[idx]\n\t" \
    "v_exp_f32 v90, v90\n\tv_exp_f32 v91, v91\n\tv_exp_f32 v92, v92\n\tv_exp_f32 v93, v93\n\t" \
    "v_exp_f32 v94, v94\n\tv_exp_f32 v95, v95\n\tv_exp_f32 v64, v64\n\tv_exp_f32 v65, v65\n\t" \
    "v_smfmac_f32_32x32x32_bf16 %[o1], %[v1a], v[88:95], %[idx]\n\ts_waitcnt lgkmcnt(0)\n\tv_exp_f32 v66, v66\n\tv_exp_f32 v67, v67\n\t" \
    "v_exp_f32 v68, v68\n\tv_mfma_f32_32x32x16_bf16 v[80:95], %[ka], %[q3], %[ci]\n\tds_read_b128 %[ka], %[ad] offset:24576\n\tv_exp_f32 v69, v69\n\t" \
    "v_exp_f32 v70, v70\n\tv_exp_f32 v71, v71\n\tv_exp_f32 v72, v72\n\tv_exp_f32 v73, v73\n\t" \
    "v_smfmac_f32_32x32x32_bf16 %[o2], %[v0a], v[64:71], %[idx]\n\tv_exp_f32 v74, v74\n\tv_exp_f32 v75, v75\n\tv_exp_f32 v76, v76\n\t" \
    "v_exp_f32 v77, v77\n\tv_exp_f32 v78, v78\n\tv_exp_f32 v79, v79\n\tv_exp_f32 v80, v80\n\t" \
    "v_exp_f32 v81, v81\n\tv_smfmac_f32_32x32x32_bf16 %[o2], %[v1a], v[72:79], %[idx]\n\tv_exp_f32 v82, v82\n\tv_exp_f32 v83, v83\n\t" \
    "v_exp_f32 v84, v84\n\tv_mfma_f32_32x32x16_bf16 v[64:79], %[kb], %[q0], %[ci]\n\tv_exp_f32 v85, v85\n\tv_exp_f32 v86, v86\n\t" \
    "v_exp_f32 v87, v87\n\tv_exp_f32 v88, v88\n\tv_exp_f32 v89, v89\n\tv_smfmac_f32_32x32x32_bf16 %[o3], %[v0a], v[80:87], %[idx]\n\t" \
    "v_exp_f32 v90, v90\n\tv_exp_f32 v91, v91\n\tv_exp_f32 v92, v92\n\tv_exp_f32 v93, v93\n\t" \
    "v_exp_f32 v94, v94\n\tv_exp_f32 v95, v95\n\tv_exp_f32 v64, v64\n\tv_exp_f32 v65, v65\n\t" \
    "v_smfmac_f32_32x32x32_bf16 %[o3], %[v1a], v[88:95], %[idx]\n\ts_mov_b64 exec, %[msk]\n\tds_read_b128 %[v1a], %[ad] offset:25856\n\tds_read_b128 %[v0a], %[ad] offset:25600\n\t" \
    "s_mov_b64 exec, -1\n\tv_exp_f32 v66, v66\n\tv_exp_f32 v67, v67\n\tv_exp_f32 v68, v68\n\t" \
    "v_mfma_f32_32x32x16_bf16 v[80:95], %[kb], %[q1], %[ci]\n\tv_exp_f32 v69, v69\n\tv_exp_f32 v70, v70\n\tv_exp_f32 v71, v71\n\t" \
    "v_exp_f32 v72, v72\n\tv_exp_f32 v73, v73\n\tv_smfmac_f32_32x32x32_bf16 %[o0], %[v0b], v[64:71], %[idx]\n\tv_exp_f32 v74, v74\n\t" \
    "v_exp_f32 v75, v75\n\tv_exp_f32 v76, v76\n\tv_exp_f32 v77, v77\n\tv_exp_f32 v78, v78\n\t" \
    "v_exp_f32 v79, v79\n\tv_exp_f32 v80, v80\n\tv_exp_f32 v81, v81\n\tv_smfmac_f32_32x32x32_bf16 %[o0], %[v1b], v[72:79], %[idx]\n\t" \
    "v_exp_f32 v82, v82\n\tv_exp_f32 v83, v83\n\tv_exp_f32 v84, v84\n\tv_mfma_f32_32x32x16_bf16 v[64:79], %[kb], %[q2], %[ci]\n\t" \
    "v_exp_f32 v85, v85\n\tv_exp_f32 v86, v86\n\tv_exp_f32 v87, v87\n\tv_exp_f32 v88, v88\n\t" \
    "v_exp_f32 v89, v89\n\tv_smfmac_f32_32x32x32_bf16 %[o1], %[v0b], v[80:87], %[idx]\n\tv_exp_f32 v90, v90\n\tv_exp_f32 v91, v91\n\t" \
    "v_exp_f32 v92, v92\n\tv_exp_f32 v93, v93\n\tv_exp_f32 v94, v94\n\tv_exp_f32 v95, v95\n\t" \
    "v_exp_f32 v64, v64\n\tv_exp_f32 v65, v65\n\tv_smfmac_f32_32x32x32_bf16 %[o1], %[v1b], v[88:95], %[idx]\n\ts_waitcnt lgkmcnt(0)\n\t" \
    "v_exp_f32 v66, v66\n\tv_exp_f32 v67, v67\n\tv_exp_f32 v68, v68\n\tv_mfma_f32_32x32x16_bf16 v[80:95], %[kb], %[q3], %[ci]\n\t" \
    "ds_read_b128 %[kb], %[ad] offset:28672\n\tv_exp_f32 v69, v69\n\tv_exp_f32 v70, v70\n\tv_exp_f32 v71, v71\n\t" \
    "v_exp_f32 v72, v72\n\tv_exp_f32 v73, v73\n\tv_smfmac_f32_32x32x32_bf16 %[o2], %[v0b], v[64:71], %[idx]\n\tv_exp_f32 v74, v74\n\t" \
    "v_exp_f32 v75, v75\n\tv_exp_f32 v76, v76\n\tv_exp_f32 v77, v77\n\tv_exp_f32 v78, v78\n\t" \
    "v_exp_f32 v79, v79\n\tv_exp_f32 v80, v80\n\tv_exp_f32 v81, v81\n\tv_smfmac_f32_32x32x32_bf16 %[o2], %[v1b], v[72:79], %[idx]\n\t" \
    "v_exp_f32 v82, v82\n\tv_exp_f32 v83, v83\n\tv_exp_f32 v84, v84\n\tv_mfma_f32_32x32x16_bf16 v[64:79], %[ka], %[q0], %[ci]\n\t" \
    "v_exp_f32 v85, v85\n\tv_exp_f32 v86, v86\n\tv_exp_f32 v87, v87\n\tv_exp_f32 v88, v88\n\t" \
    "v_exp_f32 v89, v89\n\tv_smfmac_f32_32x32x32_bf16 %[o3], %[v0b], v[80:87], %[idx]\n\ts_barrier\n\tv_exp_f32 v90, v90\n\t" \
    "v_exp_f32 v91, v91\n\tv_exp_f32 v92, v92\n\tv_exp_f32 v93, v93\n\tv_exp_f32 v94, v94\n\t" \
    "v_exp_f32 v95, v95\n\tv_exp_f32 v64, v64\n\tv_exp_f32 v65, v65\n\tv_smfmac_f32_32x32x32_bf16 %[o3], %[v1b], v[88:95], %[idx]\n\t" \
    "s_mov_b64 exec, %[msk]\n\tds_read_b128 %[v1b], %[ad] offset:29952\n\tds_read_b128 %[v0b], %[ad] offset:29696\n\ts_mov_b64 exec, -1\n\t" \
    "v_exp_f32 v66, v66\n\tv_exp_f32 v67, v67\n\tv_exp_f32 v68, v68\n\tv_mfma_f32_32x32x16_bf16 v[80:95], %[ka], %[q1], %[ci]\n\t" \
    "v_exp_f32 v69, v69\n\tv_exp_f32 v70, v70\n\tv_exp_f32 v71, v71\n\tv_exp_f32 v72, v72\n\t" \
    "v_exp_f32 v73, v73\n\tv_smfmac_f32_32x32x32_bf16 %[o0], %[v0a], v[64:71], %[idx]\n\tv_exp_f32 v74, v74\n\tv_exp_f32 v75, v75\n\t" \
    "v_exp_f32 v76, v76\n\tv_exp_f32 v77, v77\n\tv_exp_f32 v78, v78\n\tv_exp_f32 v79, v79\n\t" \
    "v_exp_f32 v80, v80\n\tv_exp_f32 v81, v81\n\tv_smfmac_f32_32x32x32_bf16 %[o0], %[v1a], v[72:79], %[idx]\n\tv_exp_f32 v82, v82\n\t" \
    "v_exp_f32 v83, v83\n\tv_exp_f32 v84, v84\n\tv_mfma_f32_32x32x16_bf16 v[64:79], %[ka], %[q2], %[ci]\n\tv_exp_f32 v85, v85\n\t" \
    "v_exp_f32 v86, v86\n\tv_exp_f32 v87, v87\n\tv_exp_f32 v88, v88\n\tv_exp_f32 v89, v89\n\t" \
    "v_smfmac_f32_32x32x32_bf16 %[o1], %[v0a], v[80:87], %[idx]\n\tv_exp_f32 v90, v90\n\tv_exp_f32 v91, v91\n\tv_exp_f32 v92, v92\n\t" \
    "v_exp_f32 v93, v93\n\tv_exp_f32 v94, v94\n\tv_exp_f32 v95, v95\n\tv_exp_f32 v64, v64\n\t" \
    "v_exp_f32 v65, v65\n\tv_smfmac_f32_32x32x32_bf16 %[o1], %[v1a], v[88:95], %[idx]\n\ts_waitcnt lgkmcnt(0)\n\tv_exp_f32 v66, v66\n\t" \
    "v_exp_f32 v67, v67\n\tv_exp_f32 v68, v68\n\tv_mfma_f32_32x32x16_bf16 v[80:95], %[ka], %[q3], %[ci]\n\tds_read_b128 %[ka], %[ad] offset:32768\n\t" \
    "v_exp_f32 v69, v69\n\tv_exp_f32 v70, v70\n\tv_exp_f32 v71, v71\n\tv_exp_f32 v72, v72\n\t" \
    "v_exp_f32 v73, v73\n\tv_smfmac_f32_32x32x32_bf16 %[o2], %[v0a], v[64:71], %[idx]\n\tv_exp_f32 v74, v74\n\tv_exp_f32 v75, v75\n\t" \
    "v_exp_f32 v76, v76\n\tv_exp_f32 v77, v77\n\tv_exp_f32 v78, v78\n\tv_exp_f32 v79, v79\n\t" \
    "v_exp_f32 v80, v80\n\tv_exp_f32 v81, v81\n\tv_smfmac_f32_32x32x32_bf16 %[o2], %[v1a], v[72:79], %[idx]\n\tv_exp_f32 v82, v82\n\t" \
    "v_exp_f32 v83, v83\n\tv_exp_f32 v84, v84\n\tv_mfma_f32_32x32x16_bf16 v[64:79], %[kb], %[q0], %[ci]\n\tv_exp_f32 v85, v85\n\t" \
    "v_exp_f32 v86, v86\n\tv_exp_f32 v87, v87\n\tv_exp_f32 v88, v88\n\tv_exp_f32 v89, v89\n\t" \
    "v_smfmac_f32_32x32x32_bf16 %[o3], %[v0a], v[80:87], %[idx]\n\tv_exp_f32 v90, v90\n\tv_exp_f32 v91, v91\n\tv_exp_f32 v92, v92\n\t" \
    "v_exp_f32 v93, v93\n\tv_exp_f32 v94, v94\n\tv_exp_f32 v95, v95\n\tv_exp_f32 v64, v64\n\t" \
    "v_exp_f32 v65, v65\n\tv_smfmac_f32_32x32x32_bf16 %[o3], %[v1a], v[88:95], %[idx]\n\ts_mov_b64 exec, %[msk]\n\tds_read_b128 %[v1a], %[ad] offset:34048\n\t" \
    "ds_read_b128 %[v0a], %[ad] offset:33792\n\ts_mov_b64 exec, -1\n\tv_exp_f32 v66, v66\n\tv_exp_f32 v67, v67\n\t" \
    "v_exp_f32 v68, v68\n\tv_mfma_f32_32x32x16_bf16 v[80:95], %[kb], %[q1], %[ci]\n\tv_exp_f32 v69, v69\n\tv_exp_f32 v70, v70\n\t" \
    "v_exp_f32 v71, v71\n\tv_exp_f32 v72, v72\n\tv_exp_f32 v73, v73\n\tv_smfmac_f32_32x32x32_bf16 %[o0], %[v0b], v[64:71], %[idx]\n\t" \
    "v_exp_f32 v74, v74\n\tv_exp_f32 v75, v75\n\tv_exp_f32 v76, v76\n\tv_exp_f32 v77, v77\n\t" \
    "v_exp_f32 v78, v78\n\tv_exp_f32 v79, v79\n\tv_exp_f32 v80, v80\n\tv_exp_f32 v81, v81\n\t" \
    "v_smfmac_f32_32x32x32_bf16 %[o0], %[v1b], v[72:79], %[idx]\n\tv_exp_f32 v82, v82\n\tv_exp_f32 v83, v83\n\tv_exp_f32 v84, v84\n\t" \
    "v_mfma_f32_32x32x16_bf16 v[64:79], %[kb], %[q2], %[ci]\n\tv_exp_f32 v85, v85\n\tv_exp_f32 v86, v86\n\tv_exp_f32 v87, v87\n\t" \
    "v_exp_f32 v88, v88\n\tv_exp_f32 v89, v89\n\tv_smfmac_f32_32x32x32_bf16 %[o1], %[v0b], v[80:87], %[idx]\n\tv_exp_f32 v90, v90\n\t" \
    "v_exp_f32 v91, v91\n\tv_exp_f32 v92, v92\n\tv_exp_f32 v93, v93\n\tv_exp_f32 v94, v94\n\t" \
    "v_exp_f32 v95, v95\n\tv_exp_f32 v64, v64\n\tv_exp_f32 v65, v65\n\tv_smfmac_f32_32x32x32_bf16 %[o1], %[v1b], v[88:95], %[idx]\n\t" \
    "s_waitcnt lgkmcnt(0)\n\tv_exp_f32 v66, v66\n\tv_exp_f32 v67, v67\n\tv_exp_f32 v68, v68\n\t" \
    "v_mfma_f32_32x32x16_bf16 v[80:95], %[kb], %[q3], %[ci]\n\tds_read_b128 %[kb], %[ad] offset:36864\n\tv_exp_f32 v69, v69\n\tv_exp_f32 v70, v70\n\t" \
    "v_exp_f32 v71, v71\n\tv_exp_f32 v72, v72\n\tv_exp_f32 v73, v73\n\tv_smfmac_f32_32x32x32_bf16 %[o2], %[v0b], v[64:71], %[idx]\n\t" \
    "v_exp_f32 v74, v74\n\tv_exp_f32 v75, v75\n\tv_exp_f32 v76, v76\n\tv_exp_f32 v77, v77\n\t" \
    "v_exp_f32 v78, v78\n\tv_exp_f32 v79, v79\n\tv_exp_f32 v80, v80\n\tv_exp_f32 v81, v81\n\t" \
    "v_smfmac_f32_32x32x32_bf16 %[o2], %[v1b], v[72:79], %[idx]\n\tv_exp_f32 v82, v82\n\tv_exp_f32 v83, v83\n\tv_exp_f32 v84, v84\n\t" \
    "v_mfma_f32_32x32x16_bf16 v[64:79], %[ka], %[q0], %[ci]\n\tv_exp_f32 v85, v85\n\tv_exp_f32 v86, v86\n\tv_exp_f32 v87, v87\n\t" \
    "v_exp_f32 v88, v88\n\tv_exp_f32 v89, v89\n\tv_smfmac_f32_32x32x32_bf16 %[o3], %[v0b], v[80:87], %[idx]\n\tv_exp_f32 v90, v90\n\t" \
    "v_exp_f32 v91, v91\n\tv_exp_f32 v92, v92\n\tv_exp_f32 v93, v93\n\tv_exp_f32 v94, v94\n\t" \
    "v_exp_f32 v95, v95\n\t"
#define PROJ_LOOP_A0 \
    "s_barrier\n\tds_read_b128 v[64:67], %[x0] offset:0\n\tds_read_b64_tr_b16 v[80:81], %[w0] offset:18432\n\tds_read_b64_tr_b16 v[82:83], %[w0] offset:19968\n\t" \
    "ds_read_b128 v[68:71], %[x0] offset:2048\n\tds_read_b64_tr_b16 v[84:85], %[w1] offset:18432\n\tds_read_b64_tr_b16 v[86:87], %[w1] offset:19968\n\tds_read_b128 v[72:75], %[x0] offset:4096\n\t" \
    "ds_read_b64_tr_b16 v[88:89], %[w0] offset:18944\n\tds_read_b64_tr_b16 v[90:91], %[w0] offset:20480\n\tds_read_b128 v[76:79], %[x0] offset:6144\n\tds_read_b64_tr_b16 v[92:93], %[w1] offset:18944\n\t" \
    "ds_read_b64_tr_b16 v[94:95], %[w1] offset:20480\n\tds_read_b64_tr_b16 v[96:97], %[w0] offset:19456\n\tds_read_b64_tr_b16 v[98:99], %[w0] offset:20992\n\tds_read_b64_tr_b16 v[100:101], %[w1] offset:19456\n\t" \
    "ds_read_b64_tr_b16 v[102:103], %[w1] offset:20992\n\tds_read_b128 v[104:107], %[r0]\n\t1:\n\ts_cmp_eq_u32 %[ph], 2\n\t" \
    "s_cselect_b32 %[dl], %[neg], %[pos]\n\ts_cselect_b32 %[ph], -1, %[ph]\n\ts_add_u32 %[ph], %[ph], 1\n\ts_waitcnt lgkmcnt(0)\n\t" \
    "v_mfma_f32_16x16x32_bf16 v[160:163], v[80:83], v[64:67], v[160:163]\n\tds_read_b128 v[108:111], %[x1] offset:0\n\tv_mfma_f32_16x16x32_bf16 v[164:167], v[80:83], v[68:71], v[164:167]\n\tds_read_b64_tr_b16 v[124:125], %[w0] offset:30720\n\t" \
    "v_mfma_f32_16x16x32_bf16 v[168:171], v[80:83], v[72:75], v[168:171]\n\tds_read_b64_tr_b16 v[126:127], %[w0] offset:32256\n\tv_mfma_f32_16x16x32_bf16 v[172:175], v[80:83], v[76:79], v[172:175]\n\tds_read_b128 v[112:115], %[x1] offset:2048\n\t" \
    "v_mfma_f32_16x16x32_bf16 v[176:179], v[84:87], v[64:67], v[176:179]\n\tds_read_b64_tr_b16 v[128:129], %[w1] offset:30720\n\tv_mfma_f32_16x16x32_bf16 v[180:183], v[84:87], v[68:71], v[180:183]\n\tds_read_b64_tr_b16 v[130:131], %[w1] offset:32256\n\t" \
    "v_mfma_f32_16x16x32_bf16 v[184:187], v[84:87], v[72:75], v[184:187]\n\tds_read_b128 v[116:119], %[x1] offset:4096\n\tv_mfma_f32_16x16x32_bf16 v[188:191], v[84:87], v[76:79], v[188:191]\n\tds_read_b64_tr_b16 v[132:133], %[w0] offset:31232\n\t" \
    "v_mfma_f32_16x16x32_bf16 v[192:195], v[88:91], v[64:67], v[192:195]\n\tds_read_b64_tr_b16 v[134:135], %[w0] offset:32768\n\tv_mfma_f32_16x16x32_bf16 v[196:199], v[88:91], v[68:71], v[196:199]\n\tds_read_b128 v[120:123], %[x1] offset:6144\n\t" \
    "v_mfma_f32_16x16x32_bf16 v[200:203], v[88:91], v[72:75], v[200:203]\n\tds_read_b64_tr_b16 v[136:137], %[w1] offset:31232\n\tv_mfma_f32_16x16x32_bf16 v[204:207], v[88:91], v[76:79], v[204:207]\n\tds_read_b64_tr_b16 v[138:139], %[w1] offset:32768\n\t" \
    "v_mfma_f32_16x16x32_bf16 v[208:211], v[92:95], v[64:67], v[208:211]\n\tds_read_b64_tr_b16 v[140:141], %[w0] offset:31744\n\tv_mfma_f32_16x16x32_bf16 v[212:215], v[92:95], v[68:71], v[212:215]\n\tds_read_b64_tr_b16 v[142:143], %[w0] offset:33280\n\t" \
    "v_mfma_f32_16x16x32_bf16 v[216:219], v[92:95], v[72:75], v[216:219]\n\tds_read_b64_tr_b16 v[144:145], %[w1] offset:31744\n\tv_mfma_f32_16x16x32_bf16 v[220:223], v[92:95], v[76:79], v[220:223]\n\tds_read_b64_tr_b16 v[146:147], %[w1] offset:33280\n\t" \
    "v_mfma_f32_16x16x32_bf16 v[224:227], v[64:67], v[96:99], v[224:227]\n\tds_read_b128 v[148:151], %[r1]\n\tv_mfma_f32_16x16x32_bf16 v[228:231], v[68:71], v[96:99], v[228:231]\n\tv_add_u32 %[x0], %[dl], %[x0]\n\t" \
    "v_mfma_f32_16x16x32_bf16 v[232:235], v[72:75], v[96:99], v[232:235]\n\tv_add_u32 %[x1], %[dl], %[x1]\n\tv_mfma_f32_16x16x32_bf16 v[236:239], v[76:79], v[96:99], v[236:239]\n\tv_add_u32 %[w0], %[dl], %[w0]\n\t" \
    "v_mfma_f32_16x16x32_bf16 v[240:243], v[64:67], v[100:103], v[240:243]\n\tv_add_u32 %[w1], %[dl], %[w1]\n\tv_mfma_f32_16x16x32_bf16 v[244:247], v[68:71], v[100:103], v[244:247]\n\tv_add_u32 %[r0], %[dl], %[r0]\n\t" \
    "v_mfma_f32_16x16x32_bf16 v[248:251], v[72:75], v[100:103], v[248:251]\n\tv_add_u32 %[r1], %[dl], %[r1]\n\tv_mfma_f32_16x16x32_bf16 v[252:255], v[76:79], v[100:103], v[252:255]\n\tv_mfma_f32_16x16x32_bf16 v[152:155], v[88:91], v[104:107], v[152:155]\n\t" \
    "v_mfma_f32_16x16x32_bf16 v[156:159], v[104:107], v[96:99], v[156:159]\n\ts_sub_u32 %[cnt], %[cnt], 1\n\ts_cmp_eq_u32 %[cnt], 0\n\ts_waitcnt lgkmcnt(0)\n\t" \
    "s_cbranch_scc1 2f\n\ts_barrier\n\tv_mfma_f32_16x16x32_bf16 v[160:163], v[124:127], v[108:111], v[160:163]\n\tds_read_b128 v[64:67], %[x0] offset:0\n\t" \
    "v_mfma_f32_16x16x32_bf16 v[164:167], v[124:127], v[112:115], v[164:167]\n\tds_read_b64_tr_b16 v[80:81], %[w0] offset:18432\n\tv_mfma_f32_16x16x32_bf16 v[168:171], v[124:127], v[116:119], v[168:171]\n\tds_read_b64_tr_b16 v[82:83], %[w0] offset:19968\n\t" \
    "v_mfma_f32_16x16x32_bf16 v[172:175], v[124:127], v[120:123], v[172:175]\n\tds_read_b128 v[68:71], %[x0] offset:2048\n\tv_mfma_f32_16x16x32_bf16 v[176:179], v[128:131], v[108:111], v[176:179]\n\tds_read_b64_tr_b16 v[84:85], %[w1] offset:18432\n\t" \
    "v_mfma_f32_16x16x32_bf16 v[180:183], v[128:131], v[112:115], v[180:183]\n\tds_read_b64_tr_b16 v[86:87], %[w1] offset:19968\n\tv_mfma_f32_16x16x32_bf16 v[184:187], v[128:131], v[116:119], v[184:187]\n\tds_read_b128 v[72:75], %[x0] offset:4096\n\t" \
    "v_mfma_f32_16x16x32_bf16 v[188:191], v[128:131], v[120:123], v[188:191]\n\tds_read_b64_tr_b16 v[88:89], %[w0] offset:18944\n\tv_mfma_f32_16x16x32_bf16 v[192:195], v[132:135], v[108:111], v[192:195]\n\tds_read_b64_tr_b16 v[90:91], %[w0] offset:20480\n\t" \
    "v_mfma_f32_16x16x32_bf16 v[196:199], v[132:135], v[112:115], v[196:199]\n\tds_read_b128 v[76:79], %[x0] offset:6144\n\tv_mfma_f32_16x16x32_bf16 v[200:203], v[132:135], v[116:119], v[200:203]\n\tds_read_b64_tr_b16 v[92:93], %[w1] offset:18944\n\t" \
    "v_mfma_f32_16x16x32_bf16 v[204:207], v[132:135], v[120:123], v[204:207]\n\tds_read_b64_tr_b16 v[94:95], %[w1] offset:20480\n\tv_mfma_f32_16x16x32_bf16 v[208:211], v[136:139], v[108:111], v[208:211]\n\tds_read_b64_tr_b16 v[96:97], %[w0] offset:19456\n\t" \
    "v_mfma_f32_16x16x32_bf16 v[212:215], v[136:139], v[112:115], v[212:215]\n\tds_read_b64_tr_b16 v[98:99], %[w0] offset:20992\n\tv_mfma_f32_16x16x32_bf16 v[216:219], v[136:139], v[116:119], v[216:219]\n\tds_read_b64_tr_b16 v[100:101], %[w1] offset:19456\n\t" \
    "v_mfma_f32_16x16x32_bf16 v[220:223], v[136:139], v[120:123], v[220:223]\n\tds_read_b64_tr_b16 v[102:103], %[w1] offset:20992\n\tv_mfma_f32_16x16x32_bf16 v[224:227], v[108:111], v[140:143], v[224:227]\n\tds_read_b128 v[104:107], %[r0]\n\t" \
    "v_mfma_f32_16x16x32_bf16 v[228:231], v[112:115], v[140:143], v[228:231]\n\tv_mfma_f32_16x16x32_bf16 v[232:235], v[116:119], v[140:143], v[232:235]\n\tv_mfma_f32_16x16x32_bf16 v[236:239], v[120:123], v[140:143], v[236:239]\n\tv_mfma_f32_16x16x32_bf16 v[240:243], v[108:111], v[144:147], v[240:243]\n\t" \
    "v_mfma_f32_16x16x32_bf16 v[244:247], v[112:115], v[144:147], v[244:247]\n\tv_mfma_f32_16x16x32_bf16 v[248:251], v[116:119], v[144:147], v[248:251]\n\tv_mfma_f32_16x16x32_bf16 v[252:255], v[120:123], v[144:147], v[252:255]\n\tv_mfma_f32_16x16x32_bf16 v[152:155], v[132:135], v[148:151], v[152:155]\n\t" \
    "v_mfma_f32_16x16x32_bf16 v[156:159], v[148:151], v[140:143], v[156:159]\n\ts_branch 1b\n\t2:\n\tv_mfma_f32_16x16x32_bf16 v[160:163], v[124:127], v[108:111], v[160:163]\n\t" \
    "v_mfma_f32_16x16x32_bf16 v[164:167], v[124:127], v[112:115], v[164:167]\n\tv_mfma_f32_16x16x32_bf16 v[168:171], v[124:127], v[116:119], v[168:171]\n\tv_mfma_f32_16x16x32_bf16 v[172:175], v[124:127], v[120:123], v[172:175]\n\tv_mfma_f32_16x16x32_bf16 v[176:179], v[128:131], v[108:111], v[176:179]\n\t" \
    "v_mfma_f32_16x16x32_bf16 v[180:183], v[128:131], v[112:115], v[180:183]\n\tv_mfma_f32_16x16x32_bf16 v[184:187], v[128:131], v[116:119], v[184:187]\n\tv_mfma_f32_16x16x32_bf16 v[188:191], v[128:131], v[120:123], v[188:191]\n\tv_mfma_f32_16x16x32_bf16 v[192:195], v[132:135], v[108:111], v[192:195]\n\t" \
    "v_mfma_f32_16x16x32_bf16 v[196:199], v[132:135], v[112:115], v[196:199]\n\tv_mfma_f32_16x16x32_bf16 v[200:203], v[132:135], v[116:119], v[200:203]\n\tv_mfma_f32_16x16x32_bf16 v[204:207], v[132:135], v[120:123], v[204:207]\n\tv_mfma_f32_16x16x32_bf16 v[208:211], v[136:139], v[108:111], v[208:211]\n\t" \
    "v_mfma_f32_16x16x32_bf16 v[212:215], v[136:139], v[112:115], v[212:215]\n\tv_mfma_f32_16x16x32_bf16 v[216:219], v[136:139], v[116:119], v[216:219]\n\tv_mfma_f32_16x16x32_bf16 v[220:223], v[136:139], v[120:123], v[220:223]\n\tv_mfma_f32_16x16x32_bf16 v[224:227], v[108:111], v[140:143], v[224:227]\n\t" \
    "v_mfma_f32_16x16x32_bf16 v[228:231], v[112:115], v[140:143], v[228:231]\n\tv_mfma_f32_16x16x32_bf16 v[232:235], v[116:119], v[140:143], v[232:235]\n\tv_mfma_f32_16x16x32_bf16 v[236:239], v[120:123], v[140:143], v[236:239]\n\tv_mfma_f32_16x16x32_bf16 v[240:243], v[108:111], v[144:147], v[240:243]\n\t" \
    "v_mfma_f32_16x16x32_bf16 v[244:247], v[112:115], v[144:147], v[244:247]\n\tv_mfma_f32_16x16x32_bf16 v[248:251], v[116:119], v[144:147], v[248:251]\n\tv_mfma_f32_16x16x32_bf16 v[252:255], v[120:123], v[144:147], v[252:255]\n\tv_mfma_f32_16x16x32_bf16 v[152:155], v[132:135], v[148:151], v[152:155]\n\t" \
    "v_mfma_f32_16x16x32_bf16 v[156:159], v[148:151], v[140:143], v[156:159]\n\ts_nop 7\n\t"
#define PROJ_LOOP_A1 \
    "s_barrier\n\tds_read_b128 v[64:67], %[x0] offset:0\n\tds_read_b64_tr_b16 v[80:81], %[w0] offset:18432\n\tds_read_b64_tr_b16 v[82:83], %[w0] offset:19968\n\t" \
    "ds_read_b128 v[68:71], %[x0] offset:2048\n\tds_read_b64_tr_b16 v[84:85], %[w1] offset:18432\n\tds_read_b64_tr_b16 v[86:87], %[w1] offset:19968\n\tds_read_b128 v[72:75], %[x0] offset:4096\n\t" \
    "ds_read_b64_tr_b16 v[88:89], %[w0] offset:18944\n\tds_read_b64_tr_b16 v[90:91], %[w0] offset:20480\n\tds_read_b128 v[76:79], %[x0] offset:6144\n\tds_read_b64_tr_b16 v[92:93], %[w1] offset:18944\n\t" \
    "ds_read_b64_tr_b16 v[94:95], %[w1] offset:20480\n\tds_read_b64_tr_b16 v[96:97], %[w0] offset:19456\n\tds_read_b64_tr_b16 v[98:99], %[w0] offset:20992\n\tds_read_b64_tr_b16 v[100:101], %[w1] offset:19456\n\t" \
    "ds_read_b64_tr_b16 v[102:103], %[w1] offset:20992\n\tds_read_b128 v[104:107], %[r0]\n\t1:\n\ts_cmp_eq_u32 %[ph], 2\n\t" \
    "s_cselect_b32 %[dl], %[neg], %[pos]\n\ts_cselect_b32 %[ph], -1, %[ph]\n\ts_add_u32 %[ph], %[ph], 1\n\ts_waitcnt lgkmcnt(0)\n\t" \
    "v_mfma_f32_16x16x32_bf16 v[160:163], v[80:83], v[64:67], v[160:163]\n\tds_read_b128 v[108:111], %[x1] offset:0\n\tv_mfma_f32_16x16x32_bf16 v[164:167], v[80:83], v[68:71], v[164:167]\n\tds_read_b64_tr_b16 v[124:125], %[w0] offset:30720\n\t" \
    "v_mfma_f32_16x16x32_bf16 v[168:171], v[80:83], v[72:75], v[168:171]\n\tds_read_b64_tr_b16 v[126:127], %[w0] offset:32256\n\tv_mfma_f32_16x16x32_bf16 v[172:175], v[80:83], v[76:79], v[172:175]\n\tds_read_b128 v[112:115], %[x1] offset:2048\n\t" \
    "v_mfma_f32_16x16x32_bf16 v[176:179], v[84:87], v[64:67], v[176:179]\n\tds_read_b64_tr_b16 v[128:129], %[w1] offset:30720\n\tv_mfma_f32_16x16x32_bf16 v[180:183], v[84:87], v[68:71], v[180:183]\n\tds_read_b64_tr_b16 v[130:131], %[w1] offset:32256\n\t" \
    "v_mfma_f32_16x16x32_bf16 v[184:187], v[84:87], v[72:75], v[184:187]\n\tds_read_b128 v[116:119], %[x1] offset:4096\n\tv_mfma_f32_16x16x32_bf16 v[188:191], v[84:87], v[76:79], v[188:191]\n\tds_read_b64_tr_b16 v[132:133], %[w0] offset:31232\n\t" \
    "v_mfma_f32_16x16x32_bf16 v[192:195], v[88:91], v[64:67], v[192:195]\n\tds_read_b64_tr_b16 v[134:135], %[w0] offset:32768\n\tv_mfma_f32_16x16x32_bf16 v[196:199], v[88:91], v[68:71], v[196:199]\n\tds_read_b128 v[120:123], %[x1] offset:6144\n\t" \
    "v_mfma_f32_16x16x32_bf16 v[200:203], v[88:91], v[72:75], v[200:203]\n\tds_read_b64_tr_b16 v[136:137], %[w1] offset:31232\n\tv_mfma_f32_16x16x32_bf16 v[204:207], v[88:91], v[76:79], v[204:207]\n\tds_read_b64_tr_b16 v[138:139], %[w1] offset:32768\n\t" \
    "v_mfma_f32_16x16x32_bf16 v[208:211], v[92:95], v[64:67], v[208:211]\n\tds_read_b64_tr_b16 v[140:141], %[w0] offset:31744\n\tv_mfma_f32_16x16x32_bf16 v[212:215], v[92:95], v[68:71], v[212:215]\n\tds_read_b64_tr_b16 v[142:143], %[w0] offset:33280\n\t" \
    "v_mfma_f32_16x16x32_bf16 v[216:219], v[92:95], v[72:75], v[216:219]\n\tds_read_b64_tr_b16 v[144:145], %[w1] offset:31744\n\tv_mfma_f32_16x16x32_bf16 v[220:223], v[92:95], v[76:79], v[220:223]\n\tds_read_b64_tr_b16 v[146:147], %[w1] offset:33280\n\t" \
    "v_mfma_f32_16x16x32_bf16 v[224:227], v[64:67], v[96:99], v[224:227]\n\tds_read_b128 v[148:151], %[r1]\n\tv_mfma_f32_16x16x32_bf16 v[228:231], v[68:71], v[96:99], v[228:231]\n\tv_add_u32 %[x0], %[dl], %[x0]\n\t" \
    "v_mfma_f32_16x16x32_bf16 v[232:235], v[72:75], v[96:99], v[232:235]\n\tv_add_u32 %[x1], %[dl], %[x1]\n\tv_mfma_f32_16x16x32_bf16 v[236:239], v[76:79], v[96:99], v[236:239]\n\tv_add_u32 %[w0], %[dl], %[w0]\n\t" \
    "v_mfma_f32_16x16x32_bf16 v[240:243], v[64:67], v[100:103], v[240:243]\n\tv_add_u32 %[w1], %[dl], %[w1]\n\tv_mfma_f32_16x16x32_bf16 v[244:247], v[68:71], v[100:103], v[244:247]\n\tv_add_u32 %[r0], %[dl], %[r0]\n\t" \
    "v_mfma_f32_16x16x32_bf16 v[248:251], v[72:75], v[100:103], v[248:251]\n\tv_add_u32 %[r1], %[dl], %[r1]\n\tv_mfma_f32_16x16x32_bf16 v[252:255], v[76:79], v[100:103], v[252:255]\n\tv_mfma_f32_16x16x32_bf16 v[152:155], v[92:95], v[104:107], v[152:155]\n\t" \
    "v_mfma_f32_16x16x32_bf16 v[156:159], v[104:107], v[100:103], v[156:159]\n\ts_sub_u32 %[cnt], %[cnt], 1\n\ts_cmp_eq_u32 %[cnt], 0\n\ts_waitcnt lgkmcnt(0)\n\t" \
    "s_cbranch_scc1 2f\n\ts_barrier\n\tv_mfma_f32_16x16x32_bf16 v[160:163], v[124:127], v[108:111], v[160:163]\n\tds_read_b128 v[64:67], %[x0] offset:0\n\t" \
    "v_mfma_f32_16x16x32_bf16 v[164:167], v[124:127], v[112:115], v[164:167]\n\tds_read_b64_tr_b16 v[80:81], %[w0] offset:18432\n\tv_mfma_f32_16x16x32_bf16 v[168:171], v[124:127], v[116:119], v[168:171]\n\tds_read_b64_tr_b16 v[82:83], %[w0] offset:19968\n\t" \
    "v_mfma_f32_16x16x32_bf16 v[172:175], v[124:127], v[120:123], v[172:175]\n\tds_read_b128 v[68:71], %[x0] offset:2048\n\tv_mfma_f32_16x16x32_bf16 v[176:179], v[128:131], v[108:111], v[176:179]\n\tds_read_b64_tr_b16 v[84:85], %[w1] offset:18432\n\t" \
    "v_mfma_f32_16x16x32_bf16 v[180:183], v[128:131], v[112:115], v[180:183]\n\tds_read_b64_tr_b16 v[86:87], %[w1] offset:19968\n\tv_mfma_f32_16x16x32_bf16 v[184:187], v[128:131], v[116:119], v[184:187]\n\tds_read_b128 v[72:75], %[x0] offset:4096\n\t" \
    "v_mfma_f32_16x16x32_bf16 v[188:191], v[128:131], v[120:123], v[188:191]\n\tds_read_b64_tr_b16 v[88:89], %[w0] offset:18944\n\tv_mfma_f32_16x16x32_bf16 v[192:195], v[132:135], v[108:111], v[192:195]\n\tds_read_b64_tr_b16 v[90:91], %[w0] offset:20480\n\t" \
    "v_mfma_f32_16x16x32_bf16 v[196:199], v[132:135], v[112:115], v[196:199]\n\tds_read_b128 v[76:79], %[x0] offset:6144\n\tv_mfma_f32_16x16x32_bf16 v[200:203], v[132:135], v[116:119], v[200:203]\n\tds_read_b64_tr_b16 v[92:93], %[w1] offset:18944\n\t" \
    "v_mfma_f32_16x16x32_bf16 v[204:207], v[132:135], v[120:123], v[204:207]\n\tds_read_b64_tr_b16 v[94:95], %[w1] offset:20480\n\tv_mfma_f32_16x16x32_bf16 v[208:211], v[136:139], v[108:111], v[208:211]\n\tds_read_b64_tr_b16 v[96:97], %[w0] offset:19456\n\t" \
    "v_mfma_f32_16x16x32_bf16 v[212:215], v[136:139], v[112:115], v[212:215]\n\tds_read_b64_tr_b16 v[98:99], %[w0] offset:20992\n\tv_mfma_f32_16x16x32_bf16 v[216:219], v[136:139], v[116:119], v[216:219]\n\tds_read_b64_tr_b16 v[100:101], %[w1] offset:19456\n\t" \
    "v_mfma_f32_16x16x32_bf16 v[220:223], v[136:139], v[120:123], v[220:223]\n\tds_read_b64_tr_b16 v[102:103], %[w1] offset:20992\n\tv_mfma_f32_16x16x32_bf16 v[224:227], v[108:111], v[140:143], v[224:227]\n\tds_read_b128 v[104:107], %[r0]\n\t" \
    "v_mfma_f32_16x16x32_bf16 v[228:231], v[112:115], v[140:143], v[228:231]\n\tv_mfma_f32_16x16x32_bf16 v[232:235], v[116:119], v[140:143], v[232:235]\n\tv_mfma_f32_16x16x32_bf16 v[236:239], v[120:123], v[140:143], v[236:239]\n\tv_mfma_f32_16x16x32_bf16 v[240:243], v[108:111], v[144:147], v[240:243]\n\t" \
    "v_mfma_f32_16x16x32_bf16 v[244:247], v[112:115], v[144:147], v[244:247]\n\tv_mfma_f32_16x16x32_bf16 v[248:251], v[116:119], v[144:147], v[248:251]\n\tv_mfma_f32_16x16x32_bf16 v[252:255], v[120:123], v[144:147], v[252:255]\n\tv_mfma_f32_16x16x32_bf16 v[152:155], v[136:139], v[148:151], v[152:155]\n\t" \
    "v_mfma_f32_16x16x32_bf16 v[156:159], v[148:151], v[144:147], v[156:159]\n\ts_branch 1b\n\t2:\n\tv_mfma_f32_16x16x32_bf16 v[160:163], v[124:127], v[108:111], v[160:163]\n\t" \
    "v_mfma_f32_16x16x32_bf16 v[164:167], v[124:127], v[112:115], v[164:167]\n\tv_mfma_f32_16x16x32_bf16 v[168:171], v[124:127], v[116:119], v[168:171]\n\tv_mfma_f32_16x16x32_bf16 v[172:175], v[124:127], v[120:123], v[172:175]\n\tv_mfma_f32_16x16x32_bf16 v[176:179], v[128:131], v[108:111], v[176:179]\n\t" \
    "v_mfma_f32_16x16x32_bf16 v[180:183], v[128:131], v[112:115], v[180:183]\n\tv_mfma_f32_16x16x32_bf16 v[184:187], v[128:131], v[116:119], v[184:187]\n\tv_mfma_f32_16x16x32_bf16 v[188:191], v[128:131], v[120:123], v[188:191]\n\tv_mfma_f32_16x16x32_bf16 v[192:195], v[132:135], v[108:111], v[192:195]\n\t" \
    "v_mfma_f32_16x16x32_bf16 v[196:199], v[132:135], v[112:115], v[196:199]\n\tv_mfma_f32_16x16x32_bf16 v[200:203], v[132:135], v[116:119], v[200:203]\n\tv_mfma_f32_16x16x32_bf16 v[204:207], v[132:135], v[120:123], v[204:207]\n\tv_mfma_f32_16x16x32_bf16 v[208:211], v[136:139], v[108:111], v[208:211]\n\t" \
    "v_mfma_f32_16x16x32_bf16 v[212:215], v[136:139], v[112:115], v[212:215]\n\tv_mfma_f32_16x16x32_bf16 v[216:219], v[136:139], v[116:119], v[216:219]\n\tv_mfma_f32_16x16x32_bf16 v[220:223], v[136:139], v[120:123], v[220:223]\n\tv_mfma_f32_16x16x32_bf16 v[224:227], v[108:111], v[140:143], v[224:227]\n\t" \
    "v_mfma_f32_16x16x32_bf16 v[228:231], v[112:115], v[140:143], v[228:231]\n\tv_mfma_f32_16x16x32_bf16 v[232:235], v[116:119], v[140:143], v[232:235]\n\tv_mfma_f32_16x16x32_bf16 v[236:239], v[120:123], v[140:143], v[236:239]\n\tv_mfma_f32_16x16x32_bf16 v[240:243], v[108:111], v[144:147], v[240:243]\n\t" \
    "v_mfma_f32_16x16x32_bf16 v[244:247], v[112:115], v[144:147], v[244:247]\n\tv_mfma_f32_16x16x32_bf16 v[248:251], v[116:119], v[144:147], v[248:251]\n\tv_mfma_f32_16x16x32_bf16 v[252:255], v[120:123], v[144:147], v[252:255]\n\tv_mfma_f32_16x16x32_bf16 v[152:155], v[136:139], v[148:151], v[152:155]\n\t" \
    "v_mfma_f32_16x16x32_bf16 v[156:159], v[148:151], v[144:147], v[156:159]\n\ts_nop 7\n\t"
#define PROJ_B_KV \
    "s_barrier\n\tds_read_b128 v[64:67], %[x0] offset:0\n\tds_read_b64_tr_b16 v[80:81], %[w0] offset:18432\n\tds_read_b64_tr_b16 v[82:83], %[w0] offset:19968\n\t" \
    "ds_read_b128 v[68:71], %[x0] offset:2048\n\tds_read_b64_tr_b16 v[84:85], %[w1] offset:18432\n\tds_read_b64_tr_b16 v[86:87], %[w1] offset:19968\n\tds_read_b128 v[72:75], %[x0] offset:4096\n\t" \
    "ds_read_b64_tr_b16 v[88:89], %[w0] offset:18944\n\tds_read_b64_tr_b16 v[90:91], %[w0] offset:20480\n\tds_read_b128 v[76:79], %[x0] offset:6144\n\tds_read_b64_tr_b16 v[92:93], %[w1] offset:18944\n\t" \
    "ds_read_b64_tr_b16 v[94:95], %[w1] offset:20480\n\tds_read_b64_tr_b16 v[96:97], %[w0] offset:19456\n\tds_read_b64_tr_b16 v[98:99], %[w0] offset:20992\n\tds_read_b64_tr_b16 v[100:101], %[w1] offset:19456\n\t" \
    "ds_read_b64_tr_b16 v[102:103], %[w1] offset:20992\n\t1:\n\ts_cmp_eq_u32 %[ph], 2\n\ts_cselect_b32 %[dl], %[neg], %[pos]\n\t" \
    "s_cselect_b32 %[ph], -1, %[ph]\n\ts_add_u32 %[ph], %[ph], 1\n\ts_waitcnt lgkmcnt(0)\n\tv_mfma_f32_16x16x32_bf16 v[160:163], v[80:83], v[64:67], v[160:163]\n\t" \
    "ds_read_b128 v[108:111], %[x1] offset:0\n\tv_mfma_f32_16x16x32_bf16 v[164:167], v[80:83], v[68:71], v[164:167]\n\tds_read_b64_tr_b16 v[124:125], %[w0] offset:30720\n\tv_mfma_f32_16x16x32_bf16 v[168:171], v[80:83], v[72:75], v[168:171]\n\t" \
    "ds_read_b64_tr_b16 v[126:127], %[w0] offset:32256\n\tv_mfma_f32_16x16x32_bf16 v[172:175], v[80:83], v[76:79], v[172:175]\n\tds_read_b128 v[112:115], %[x1] offset:2048\n\tv_mfma_f32_16x16x32_bf16 v[176:179], v[84:87], v[64:67], v[176:179]\n\t" \
    "ds_read_b64_tr_b16 v[128:129], %[w1] offset:30720\n\tv_mfma_f32_16x16x32_bf16 v[180:183], v[84:87], v[68:71], v[180:183]\n\tds_read_b64_tr_b16 v[130:131], %[w1] offset:32256\n\tv_mfma_f32_16x16x32_bf16 v[184:187], v[84:87], v[72:75], v[184:187]\n\t" \
    "ds_read_b128 v[116:119], %[x1] offset:4096\n\tv_mfma_f32_16x16x32_bf16 v[188:191], v[84:87], v[76:79], v[188:191]\n\tds_read_b64_tr_b16 v[132:133], %[w0] offset:31232\n\tv_mfma_f32_16x16x32_bf16 v[192:195], v[88:91], v[64:67], v[192:195]\n\t" \
    "ds_read_b64_tr_b16 v[134:135], %[w0] offset:32768\n\tv_mfma_f32_16x16x32_bf16 v[196:199], v[88:91], v[68:71], v[196:199]\n\tds_read_b128 v[120:123], %[x1] offset:6144\n\tv_mfma_f32_16x16x32_bf16 v[200:203], v[88:91], v[72:75], v[200:203]\n\t" \
    "ds_read_b64_tr_b16 v[136:137], %[w1] offset:31232\n\tv_mfma_f32_16x16x32_bf16 v[204:207], v[88:91], v[76:79], v[204:207]\n\tds_read_b64_tr_b16 v[138:139], %[w1] offset:32768\n\tv_mfma_f32_16x16x32_bf16 v[208:211], v[92:95], v[64:67], v[208:211]\n\t" \
    "ds_read_b64_tr_b16 v[140:141], %[w0] offset:31744\n\tv_mfma_f32_16x16x32_bf16 v[212:215], v[92:95], v[68:71], v[212:215]\n\tds_read_b64_tr_b16 v[142:143], %[w0] offset:33280\n\tv_mfma_f32_16x16x32_bf16 v[216:219], v[92:95], v[72:75], v[216:219]\n\t" \
    "ds_read_b64_tr_b16 v[144:145], %[w1] offset:31744\n\tv_mfma_f32_16x16x32_bf16 v[220:223], v[92:95], v[76:79], v[220:223]\n\tds_read_b64_tr_b16 v[146:147], %[w1] offset:33280\n\tv_mfma_f32_16x16x32_bf16 v[224:227], v[64:67], v[96:99], v[224:227]\n\t" \
    "v_add_u32 %[x0], %[dl], %[x0]\n\tv_mfma_f32_16x16x32_bf16 v[228:231], v[68:71], v[96:99], v[228:231]\n\tv_add_u32 %[x1], %[dl], %[x1]\n\tv_mfma_f32_16x16x32_bf16 v[232:235], v[72:75], v[96:99], v[232:235]\n\t" \
    "v_add_u32 %[w0], %[dl], %[w0]\n\tv_mfma_f32_16x16x32_bf16 v[236:239], v[76:79], v[96:99], v[236:239]\n\tv_add_u32 %[w1], %[dl], %[w1]\n\tv_mfma_f32_16x16x32_bf16 v[240:243], v[64:67], v[100:103], v[240:243]\n\t" \
    "v_mfma_f32_16x16x32_bf16 v[244:247], v[68:71], v[100:103], v[244:247]\n\tv_mfma_f32_16x16x32_bf16 v[248:251], v[72:75], v[100:103], v[248:251]\n\tv_mfma_f32_16x16x32_bf16 v[252:255], v[76:79], v[100:103], v[252:255]\n\ts_sub_u32 %[cnt], %[cnt], 1\n\t" \
    "s_cmp_lg_u32 %[cnt], 0\n\ts_waitcnt lgkmcnt(0)\n\ts_barrier\n\tv_mfma_f32_16x16x32_bf16 v[160:163], v[124:127], v[108:111], v[160:163]\n\t" \
    "ds_read_b128 v[64:67], %[x0] offset:0\n\tv_mfma_f32_16x16x32_bf16 v[164:167], v[124:127], v[112:115], v[164:167]\n\tds_read_b64_tr_b16 v[80:81], %[w0] offset:18432\n\tv_mfma_f32_16x16x32_bf16 v[168:171], v[124:127], v[116:119], v[168:171]\n\t" \
    "ds_read_b64_tr_b16 v[82:83], %[w0] offset:19968\n\tv_mfma_f32_16x16x32_bf16 v[172:175], v[124:127], v[120:123], v[172:175]\n\tds_read_b128 v[68:71], %[x0] offset:2048\n\tv_mfma_f32_16x16x32_bf16 v[176:179], v[128:131], v[108:111], v[176:179]\n\t" \
    "ds_read_b64_tr_b16 v[84:85], %[w1] offset:18432\n\tv_mfma_f32_16x16x32_bf16 v[180:183], v[128:131], v[112:115], v[180:183]\n\tds_read_b64_tr_b16 v[86:87], %[w1] offset:19968\n\tv_mfma_f32_16x16x32_bf16 v[184:187], v[128:131], v[116:119], v[184:187]\n\t" \
    "ds_read_b128 v[72:75], %[x0] offset:4096\n\tv_mfma_f32_16x16x32_bf16 v[188:191], v[128:131], v[120:123], v[188:191]\n\tds_read_b64_tr_b16 v[88:89], %[w0] offset:18944\n\tv_mfma_f32_16x16x32_bf16 v[192:195], v[132:135], v[108:111], v[192:195]\n\t" \
    "ds_read_b64_tr_b16 v[90:91], %[w0] offset:20480\n\tv_mfma_f32_16x16x32_bf16 v[196:199], v[132:135], v[112:115], v[196:199]\n\tds_read_b128 v[76:79], %[x0] offset:6144\n\tv_mfma_f32_16x16x32_bf16 v[200:203], v[132:135], v[116:119], v[200:203]\n\t" \
    "ds_read_b64_tr_b16 v[92:93], %[w1] offset:18944\n\tv_mfma_f32_16x16x32_bf16 v[204:207], v[132:135], v[120:123], v[204:207]\n\tds_read_b64_tr_b16 v[94:95], %[w1] offset:20480\n\tv_mfma_f32_16x16x32_bf16 v[208:211], v[136:139], v[108:111], v[208:211]\n\t" \
    "ds_read_b64_tr_b16 v[96:97], %[w0] offset:19456\n\tv_mfma_f32_16x16x32_bf16 v[212:215], v[136:139], v[112:115], v[212:215]\n\tds_read_b64_tr_b16 v[98:99], %[w0] offset:20992\n\tv_mfma_f32_16x16x32_bf16 v[216:219], v[136:139], v[116:119], v[216:219]\n\t" \
    "ds_read_b64_tr_b16 v[100:101], %[w1] offset:19456\n\tv_mfma_f32_16x16x32_bf16 v[220:223], v[136:139], v[120:123], v[220:223]\n\tds_read_b64_tr_b16 v[102:103], %[w1] offset:20992\n\tv_mfma_f32_16x16x32_bf16 v[224:227], v[108:111], v[140:143], v[224:227]\n\t" \
    "v_mfma_f32_16x16x32_bf16 v[228:231], v[112:115], v[140:143], v[228:231]\n\tv_mfma_f32_16x16x32_bf16 v[232:235], v[116:119], v[140:143], v[232:235]\n\tv_mfma_f32_16x16x32_bf16 v[236:239], v[120:123], v[140:143], v[236:239]\n\tv_mfma_f32_16x16x32_bf16 v[240:243], v[108:111], v[144:147], v[240:243]\n\t" \
    "v_mfma_f32_16x16x32_bf16 v[244:247], v[112:115], v[144:147], v[244:247]\n\tv_mfma_f32_16x16x32_bf16 v[248:251], v[116:119], v[144:147], v[248:251]\n\tv_mfma_f32_16x16x32_bf16 v[252:255], v[120:123], v[144:147], v[252:255]\n\ts_cbranch_scc1 1b\n\t" \
    "s_waitcnt lgkmcnt(0)\n\tv_mfma_f32_16x16x32_bf16 v[192:195], v[88:91], v[64:67], v[192:195]\n\tds_read_b128 v[108:111], %[x1] offset:0\n\tv_mfma_f32_16x16x32_bf16 v[196:199], v[88:91], v[68:71], v[196:199]\n\t" \
    "ds_read_b128 v[112:115], %[x1] offset:2048\n\tv_mfma_f32_16x16x32_bf16 v[200:203], v[88:91], v[72:75], v[200:203]\n\tds_read_b128 v[116:119], %[x1] offset:4096\n\tv_mfma_f32_16x16x32_bf16 v[204:207], v[88:91], v[76:79], v[204:207]\n\t" \
    "ds_read_b64_tr_b16 v[132:133], %[w0] offset:31232\n\tv_mfma_f32_16x16x32_bf16 v[208:211], v[92:95], v[64:67], v[208:211]\n\tds_read_b64_tr_b16 v[134:135], %[w0] offset:32768\n\tv_mfma_f32_16x16x32_bf16 v[212:215], v[92:95], v[68:71], v[212:215]\n\t" \
    "ds_read_b128 v[120:123], %[x1] offset:6144\n\tv_mfma_f32_16x16x32_bf16 v[216:219], v[92:95], v[72:75], v[216:219]\n\tds_read_b64_tr_b16 v[136:137], %[w1] offset:31232\n\tv_mfma_f32_16x16x32_bf16 v[220:223], v[92:95], v[76:79], v[220:223]\n\t" \
    "ds_read_b64_tr_b16 v[138:139], %[w1] offset:32768\n\tv_mfma_f32_16x16x32_bf16 v[224:227], v[64:67], v[96:99], v[224:227]\n\tds_read_b64_tr_b16 v[140:141], %[w0] offset:31744\n\tv_mfma_f32_16x16x32_bf16 v[228:231], v[68:71], v[96:99], v[228:231]\n\t" \
    "ds_read_b64_tr_b16 v[142:143], %[w0] offset:33280\n\tv_mfma_f32_16x16x32_bf16 v[232:235], v[72:75], v[96:99], v[232:235]\n\tds_read_b64_tr_b16 v[144:145], %[w1] offset:31744\n\tv_mfma_f32_16x16x32_bf16 v[236:239], v[76:79], v[96:99], v[236:239]\n\t" \
    "ds_read_b64_tr_b16 v[146:147], %[w1] offset:33280\n\tv_mfma_f32_16x16x32_bf16 v[240:243], v[64:67], v[100:103], v[240:243]\n\tv_add_u32 %[x0], %[neg], %[x0]\n\tv_mfma_f32_16x16x32_bf16 v[244:247], v[68:71], v[100:103], v[244:247]\n\t" \
    "v_add_u32 %[x1], %[neg], %[x1]\n\tv_mfma_f32_16x16x32_bf16 v[248:251], v[72:75], v[100:103], v[248:251]\n\tv_add_u32 %[w0], %[neg], %[w0]\n\tv_mfma_f32_16x16x32_bf16 v[252:255], v[76:79], v[100:103], v[252:255]\n\t" \
    "v_add_u32 %[w1], %[neg], %[w1]\n\ts_waitcnt lgkmcnt(0)\n\ts_barrier\n\tv_mfma_f32_16x16x32_bf16 v[192:195], v[132:135], v[108:111], v[192:195]\n\t" \
    "ds_read_b128 v[64:67], %[x0] offset:0\n\tv_mfma_f32_16x16x32_bf16 v[196:199], v[132:135], v[112:115], v[196:199]\n\tds_read_b128 v[68:71], %[x0] offset:2048\n\tv_mfma_f32_16x16x32_bf16 v[200:203], v[132:135], v[116:119], v[200:203]\n\t" \
    "ds_read_b128 v[72:75], %[x0] offset:4096\n\tv_mfma_f32_16x16x32_bf16 v[204:207], v[132:135], v[120:123], v[204:207]\n\tds_read_b64_tr_b16 v[88:89], %[w0] offset:18944\n\tv_mfma_f32_16x16x32_bf16 v[208:211], v[136:139], v[108:111], v[208:211]\n\t" \
    "ds_read_b64_tr_b16 v[90:91], %[w0] offset:20480\n\tv_mfma_f32_16x16x32_bf16 v[212:215], v[136:139], v[112:115], v[212:215]\n\tds_read_b128 v[76:79], %[x0] offset:6144\n\tv_mfma_f32_16x16x32_bf16 v[216:219], v[136:139], v[116:119], v[216:219]\n\t" \
    "ds_read_b64_tr_b16 v[92:93], %[w1] offset:18944\n\tv_mfma_f32_16x16x32_bf16 v[220:223], v[136:139], v[120:123], v[220:223]\n\tds_read_b64_tr_b16 v[94:95], %[w1] offset:20480\n\tv_mfma_f32_16x16x32_bf16 v[224:227], v[108:111], v[140:143], v[224:227]\n\t" \
    "ds_read_b64_tr_b16 v[96:97], %[w0] offset:19456\n\tv_mfma_f32_16x16x32_bf16 v[228:231], v[112:115], v[140:143], v[228:231]\n\tds_read_b64_tr_b16 v[98:99], %[w0] offset:20992\n\tv_mfma_f32_16x16x32_bf16 v[232:235], v[116:119], v[140:143], v[232:235]\n\t" \
    "ds_read_b64_tr_b16 v[100:101], %[w1] offset:19456\n\tv_mfma_f32_16x16x32_bf16 v[236:239], v[120:123], v[140:143], v[236:239]\n\tds_read_b64_tr_b16 v[102:103], %[w1] offset:20992\n\tv_mfma_f32_16x16x32_bf16 v[240:243], v[108:111], v[144:147], v[240:243]\n\t" \
    "v_mfma_f32_16x16x32_bf16 v[244:247], v[112:115], v[144:147], v[244:247]\n\tv_mfma_f32_16x16x32_bf16 v[248:251], v[116:119], v[144:147], v[248:251]\n\tv_mfma_f32_16x16x32_bf16 v[252:255], v[120:123], v[144:147], v[252:255]\n\ts_waitcnt lgkmcnt(0)\n\t" \
    "v_mfma_f32_16x16x32_bf16 v[192:195], v[88:91], v[64:67], v[192:195]\n\tds_read_b128 v[108:111], %[x1] offset:0\n\tv_mfma_f32_16x16x32_bf16 v[196:199], v[88:91], v[68:71], v[196:199]\n\tds_read_b128 v[112:115], %[x1] offset:2048\n\t" \
    "v_mfma_f32_16x16x32_bf16 v[200:203], v[88:91], v[72:75], v[200:203]\n\tds_read_b128 v[116:119], %[x1] offset:4096\n\tv_mfma_f32_16x16x32_bf16 v[204:207], v[88:91], v[76:79], v[204:207]\n\tds_read_b64_tr_b16 v[132:133], %[w0] offset:31232\n\t" \
    "v_mfma_f32_16x16x32_bf16 v[208:211], v[92:95], v[64:67], v[208:211]\n\tds_read_b64_tr_b16 v[134:135], %[w0] offset:32768\n\tv_mfma_f32_16x16x32_bf16 v[212:215], v[92:95], v[68:71], v[212:215]\n\tds_read_b128 v[120:123], %[x1] offset:6144\n\t" \
    "v_mfma_f32_16x16x32_bf16 v[216:219], v[92:95], v[72:75], v[216:219]\n\tds_read_b64_tr_b16 v[136:137], %[w1] offset:31232\n\tv_mfma_f32_16x16x32_bf16 v[220:223], v[92:95], v[76:79], v[220:223]\n\tds_read_b64_tr_b16 v[138:139], %[w1] offset:32768\n\t" \
    "v_mfma_f32_16x16x32_bf16 v[224:227], v[64:67], v[96:99], v[224:227]\n\tds_read_b64_tr_b16 v[140:141], %[w0] offset:31744\n\tv_mfma_f32_16x16x32_bf16 v[228:231], v[68:71], v[96:99], v[228:231]\n\tds_read_b64_tr_b16 v[142:143], %[w0] offset:33280\n\t" \
    "v_mfma_f32_16x16x32_bf16 v[232:235], v[72:75], v[96:99], v[232:235]\n\tds_read_b64_tr_b16 v[144:145], %[w1] offset:31744\n\tv_mfma_f32_16x16x32_bf16 v[236:239], v[76:79], v[96:99], v[236:239]\n\tds_read_b64_tr_b16 v[146:147], %[w1] offset:33280\n\t" \
    "v_mfma_f32_16x16x32_bf16 v[240:243], v[64:67], v[100:103], v[240:243]\n\tv_add_u32 %[x0], %[pos], %[x0]\n\tv_mfma_f32_16x16x32_bf16 v[244:247], v[68:71], v[100:103], v[244:247]\n\tv_add_u32 %[x1], %[pos], %[x1]\n\t" \
    "v_mfma_f32_16x16x32_bf16 v[248:251], v[72:75], v[100:103], v[248:251]\n\tv_add_u32 %[w0], %[pos], %[w0]\n\tv_mfma_f32_16x16x32_bf16 v[252:255], v[76:79], v[100:103], v[252:255]\n\tv_add_u32 %[w1], %[pos], %[w1]\n\t" \
    "s_waitcnt lgkmcnt(0)\n\ts_barrier\n\tv_mfma_f32_16x16x32_bf16 v[192:195], v[132:135], v[108:111], v[192:195]\n\tds_read_b128 v[64:67], %[x0] offset:0\n\t" \
    "v_mfma_f32_16x16x32_bf16 v[196:199], v[132:135], v[112:115], v[196:199]\n\tds_read_b128 v[68:71], %[x0] offset:2048\n\tv_mfma_f32_16x16x32_bf16 v[200:203], v[132:135], v[116:119], v[200:203]\n\tds_read_b128 v[72:75], %[x0] offset:4096\n\t" \
    "v_mfma_f32_16x16x32_bf16 v[204:207], v[132:135], v[120:123], v[204:207]\n\tds_read_b64_tr_b16 v[88:89], %[w0] offset:18944\n\tv_mfma_f32_16x16x32_bf16 v[208:211], v[136:139], v[108:111], v[208:211]\n\tds_read_b64_tr_b16 v[90:91], %[w0] offset:20480\n\t" \
    "v_mfma_f32_16x16x32_bf16 v[212:215], v[136:139], v[112:115], v[212:215]\n\tds_read_b128 v[76:79], %[x0] offset:6144\n\tv_mfma_f32_16x16x32_bf16 v[216:219], v[136:139], v[116:119], v[216:219]\n\tds_read_b64_tr_b16 v[92:93], %[w1] offset:18944\n\t" \
    "v_mfma_f32_16x16x32_bf16 v[220:223], v[136:139], v[120:123], v[220:223]\n\tds_read_b64_tr_b16 v[94:95], %[w1] offset:20480\n\tv_mfma_f32_16x16x32_bf16 v[224:227], v[108:111], v[140:143], v[224:227]\n\tds_read_b64_tr_b16 v[96:97], %[w0] offset:19456\n\t" \
    "v_mfma_f32_16x16x32_bf16 v[228:231], v[112:115], v[140:143], v[228:231]\n\tds_read_b64_tr_b16 v[98:99], %[w0] offset:20992\n\tv_mfma_f32_16x16x32_bf16 v[232:235], v[116:119], v[140:143], v[232:235]\n\tds_read_b64_tr_b16 v[100:101], %[w1] offset:19456\n\t" \
    "v_mfma_f32_16x16x32_bf16 v[236:239], v[120:123], v[140:143], v[236:239]\n\tds_read_b64_tr_b16 v[102:103], %[w1] offset:20992\n\tv_mfma_f32_16x16x32_bf16 v[240:243], v[108:111], v[144:147], v[240:243]\n\tv_mfma_f32_16x16x32_bf16 v[244:247], v[112:115], v[144:147], v[244:247]\n\t" \
    "v_mfma_f32_16x16x32_bf16 v[248:251], v[116:119], v[144:147], v[248:251]\n\tv_mfma_f32_16x16x32_bf16 v[252:255], v[120:123], v[144:147], v[252:255]\n\ts_waitcnt lgkmcnt(0)\n\tv_mfma_f32_16x16x32_bf16 v[192:195], v[88:91], v[64:67], v[192:195]\n\t" \
    "ds_read_b128 v[108:111], %[x1] offset:0\n\tv_mfma_f32_16x16x32_bf16 v[196:199], v[88:91], v[68:71], v[196:199]\n\tds_read_b128 v[112:115], %[x1] offset:2048\n\tv_mfma_f32_16x16x32_bf16 v[200:203], v[88:91], v[72:75], v[200:203]\n\t" \
    "ds_read_b128 v[116:119], %[x1] offset:4096\n\tv_mfma_f32_16x16x32_bf16 v[204:207], v[88:91], v[76:79], v[204:207]\n\tds_read_b64_tr_b16 v[132:133], %[w0] offset:31232\n\tv_mfma_f32_16x16x32_bf16 v[208:211], v[92:95], v[64:67], v[208:211]\n\t" \
    "ds_read_b64_tr_b16 v[134:135], %[w0] offset:32768\n\tv_mfma_f32_16x16x32_bf16 v[212:215], v[92:95], v[68:71], v[212:215]\n\tds_read_b128 v[120:123], %[x1] offset:6144\n\tv_mfma_f32_16x16x32_bf16 v[216:219], v[92:95], v[72:75], v[216:219]\n\t" \
    "ds_read_b64_tr_b16 v[136:137], %[w1] offset:31232\n\tv_mfma_f32_16x16x32_bf16 v[220:223], v[92:95], v[76:79], v[220:223]\n\tds_read_b64_tr_b16 v[138:139], %[w1] offset:32768\n\tv_mfma_f32_16x16x32_bf16 v[224:227], v[64:67], v[96:99], v[224:227]\n\t" \
    "ds_read_b64_tr_b16 v[140:141], %[w0] offset:31744\n\tv_mfma_f32_16x16x32_bf16 v[228:231], v[68:71], v[96:99], v[228:231]\n\tds_read_b64_tr_b16 v[142:143], %[w0] offset:33280\n\tv_mfma_f32_16x16x32_bf16 v[232:235], v[72:75], v[96:99], v[232:235]\n\t" \
    "ds_read_b64_tr_b16 v[144:145], %[w1] offset:31744\n\tv_mfma_f32_16x16x32_bf16 v[236:239], v[76:79], v[96:99], v[236:239]\n\tds_read_b64_tr_b16 v[146:147], %[w1] offset:33280\n\tv_mfma_f32_16x16x32_bf16 v[240:243], v[64:67], v[100:103], v[240:243]\n\t" \
    "v_mfma_f32_16x16x32_bf16 v[244:247], v[68:71], v[100:103], v[244:247]\n\tv_mfma_f32_16x16x32_bf16 v[248:251], v[72:75], v[100:103], v[248:251]\n\tv_mfma_f32_16x16x32_bf16 v[252:255], v[76:79], v[100:103], v[252:255]\n\ts_waitcnt lgkmcnt(0)\n\t" \
    "v_mfma_f32_16x16x32_bf16 v[192:195], v[132:135], v[108:111], v[192:195]\n\tv_mfma_f32_16x16x32_bf16 v[196:199], v[132:135], v[112:115], v[196:199]\n\tv_mfma_f32_16x16x32_bf16 v[200:203], v[132:135], v[116:119], v[200:203]\n\tv_mfma_f32_16x16x32_bf16 v[204:207], v[132:135], v[120:123], v[204:207]\n\t" \
    "v_mfma_f32_16x16x32_bf16 v[208:211], v[136:139], v[108:111], v[208:211]\n\tv_mfma_f32_16x16x32_bf16 v[212:215], v[136:139], v[112:115], v[212:215]\n\tv_mfma_f32_16x16x32_bf16 v[216:219], v[136:139], v[116:119], v[216:219]\n\tv_mfma_f32_16x16x32_bf16 v[220:223], v[136:139], v[120:123], v[220:223]\n\t" \
    "v_mfma_f32_16x16x32_bf16 v[224:227], v[108:111], v[140:143], v[224:227]\n\tv_mfma_f32_16x16x32_bf16 v[228:231], v[112:115], v[140:143], v[228:231]\n\tv_mfma_f32_16x16x32_bf16 v[232:235], v[116:119], v[140:143], v[232:235]\n\tv_mfma_f32_16x16x32_bf16 v[236:239], v[120:123], v[140:143], v[236:239]\n\t" \
    "v_mfma_f32_16x16x32_bf16 v[240:243], v[108:111], v[144:147], v[240:243]\n\tv_mfma_f32_16x16x32_bf16 v[244:247], v[112:115], v[144:147], v[244:247]\n\tv_mfma_f32_16x16x32_bf16 v[248:251], v[116:119], v[144:147], v[248:251]\n\tv_mfma_f32_16x16x32_bf16 v[252:255], v[120:123], v[144:147], v[252:255]\n\t" \
    "s_nop 7\n\t"
#define PROJ_B_Q \
    "ds_read_b128 v[64:67], %[x0h] offset:0\n\tds_read_b64_tr_b16 v[80:81], %[w0h] offset:0\n\tds_read_b64_tr_b16 v[82:83], %[w0h] offset:1536\n\tds_read_b128 v[68:71], %[x0h] offset:2048\n\t" \
    "ds_read_b128 v[72:75], %[x0h] offset:4096\n\tds_read_b128 v[76:79], %[x0h] offset:6144\n\tds_read_b64_tr_b16 v[84:85], %[w1h] offset:0\n\tds_read_b64_tr_b16 v[86:87], %[w1h] offset:1536\n\t" \
    "ds_read_b128 v[88:91], %[x1h] offset:0\n\tds_read_b64_tr_b16 v[104:105], %[w0h] offset:12288\n\tds_read_b64_tr_b16 v[106:107], %[w0h] offset:13824\n\tds_read_b128 v[92:95], %[x1h] offset:2048\n\t" \
    "ds_read_b128 v[96:99], %[x1h] offset:4096\n\tds_read_b128 v[100:103], %[x1h] offset:6144\n\tds_read_b64_tr_b16 v[108:109], %[w1h] offset:12288\n\tds_read_b64_tr_b16 v[110:111], %[w1h] offset:13824\n\t" \
    "s_waitcnt lgkmcnt(8)\n\tv_mfma_f32_16x16x32_bf16 v[160:163], v[80:83], v[64:67], v[160:163]\n\tds_read_b128 v[112:115], %[x0] offset:0\n\tv_mfma_f32_16x16x32_bf16 v[164:167], v[80:83], v[68:71], v[164:167]\n\t" \
    "ds_read_b64_tr_b16 v[128:129], %[w0] offset:0\n\tv_mfma_f32_16x16x32_bf16 v[168:171], v[80:83], v[72:75], v[168:171]\n\tds_read_b64_tr_b16 v[130:131], %[w0] offset:1536\n\tv_mfma_f32_16x16x32_bf16 v[172:175], v[80:83], v[76:79], v[172:175]\n\t" \
    "ds_read_b128 v[116:119], %[x0] offset:2048\n\tv_mfma_f32_16x16x32_bf16 v[176:179], v[84:87], v[64:67], v[176:179]\n\tds_read_b128 v[120:123], %[x0] offset:4096\n\tv_mfma_f32_16x16x32_bf16 v[180:183], v[84:87], v[68:71], v[180:183]\n\t" \
    "ds_read_b128 v[124:127], %[x0] offset:6144\n\tv_mfma_f32_16x16x32_bf16 v[184:187], v[84:87], v[72:75], v[184:187]\n\tds_read_b64_tr_b16 v[132:133], %[w1] offset:0\n\tv_mfma_f32_16x16x32_bf16 v[188:191], v[84:87], v[76:79], v[188:191]\n\t" \
    "ds_read_b64_tr_b16 v[134:135], %[w1] offset:1536\n\ts_waitcnt lgkmcnt(8)\n\tv_mfma_f32_16x16x32_bf16 v[160:163], v[104:107], v[88:91], v[160:163]\n\tds_read_b128 v[64:67], %[x1] offset:0\n\t" \
    "v_mfma_f32_16x16x32_bf16 v[164:167], v[104:107], v[92:95], v[164:167]\n\tds_read_b64_tr_b16 v[80:81], %[w0] offset:12288\n\tv_mfma_f32_16x16x32_bf16 v[168:171], v[104:107], v[96:99], v[168:171]\n\tds_read_b64_tr_b16 v[82:83], %[w0] offset:13824\n\t" \
    "v_mfma_f32_16x16x32_bf16 v[172:175], v[104:107], v[100:103], v[172:175]\n\tds_read_b128 v[68:71], %[x1] offset:2048\n\tv_mfma_f32_16x16x32_bf16 v[176:179], v[108:111], v[88:91], v[176:179]\n\tds_read_b128 v[72:75], %[x1] offset:4096\n\t" \
    "v_mfma_f32_16x16x32_bf16 v[180:183], v[108:111], v[92:95], v[180:183]\n\tds_read_b128 v[76:79], %[x1] offset:6144\n\tv_mfma_f32_16x16x32_bf16 v[184:187], v[108:111], v[96:99], v[184:187]\n\tds_read_b64_tr_b16 v[84:85], %[w1] offset:12288\n\t" \
    "v_mfma_f32_16x16x32_bf16 v[188:191], v[108:111], v[100:103], v[188:191]\n\tds_read_b64_tr_b16 v[86:87], %[w1] offset:13824\n\ts_waitcnt lgkmcnt(8)\n\tv_mfma_f32_16x16x32_bf16 v[160:163], v[128:131], v[112:115], v[160:163]\n\t" \
    "ds_read_b128 v[88:91], %[x0] offset:43008\n\tv_mfma_f32_16x16x32_bf16 v[164:167], v[128:131], v[116:119], v[164:167]\n\tds_read_b64_tr_b16 v[104:105], %[w0] offset:43008\n\tv_mfma_f32_16x16x32_bf16 v[168:171], v[128:131], v[120:123], v[168:171]\n\t" \
    "ds_read_b64_tr_b16 v[106:107], %[w0] offset:44544\n\tv_mfma_f32_16x16x32_bf16 v[172:175], v[128:131], v[124:127], v[172:175]\n\tds_read_b128 v[92:95], %[x0] offset:45056\n\tv_mfma_f32_16x16x32_bf16 v[176:179], v[132:135], v[112:115], v[176:179]\n\t" \
    "ds_read_b128 v[96:99], %[x0] offset:47104\n\tv_mfma_f32_16x16x32_bf16 v[180:183], v[132:135], v[116:119], v[180:183]\n\tds_read_b128 v[100:103], %[x0] offset:49152\n\tv_mfma_f32_16x16x32_bf16 v[184:187], v[132:135], v[120:123], v[184:187]\n\t" \
    "ds_read_b64_tr_b16 v[108:109], %[w1] offset:43008\n\tv_mfma_f32_16x16x32_bf16 v[188:191], v[132:135], v[124:127], v[188:191]\n\tds_read_b64_tr_b16 v[110:111], %[w1] offset:44544\n\ts_waitcnt lgkmcnt(8)\n\t" \
    "v_mfma_f32_16x16x32_bf16 v[160:163], v[80:83], v[64:67], v[160:163]\n\tds_read_b128 v[112:115], %[x1] offset:43008\n\tv_mfma_f32_16x16x32_bf16 v[164:167], v[80:83], v[68:71], v[164:167]\n\tds_read_b64_tr_b16 v[128:129], %[w0] offset:55296\n\t" \
    "v_mfma_f32_16x16x32_bf16 v[168:171], v[80:83], v[72:75], v[168:171]\n\tds_read_b64_tr_b16 v[130:131], %[w0] offset:56832\n\tv_mfma_f32_16x16x32_bf16 v[172:175], v[80:83], v[76:79], v[172:175]\n\tds_read_b128 v[116:119], %[x1] offset:45056\n\t" \
    "v_mfma_f32_16x16x32_bf16 v[176:179], v[84:87], v[64:67], v[176:179]\n\tds_read_b128 v[120:123], %[x1] offset:47104\n\tv_mfma_f32_16x16x32_bf16 v[180:183], v[84:87], v[68:71], v[180:183]\n\tds_read_b128 v[124:127], %[x1] offset:49152\n\t" \
    "v_mfma_f32_16x16x32_bf16 v[184:187], v[84:87], v[72:75], v[184:187]\n\tds_read_b64_tr_b16 v[132:133], %[w1] offset:55296\n\tv_mfma_f32_16x16x32_bf16 v[188:191], v[84:87], v[76:79], v[188:191]\n\tds_read_b64_tr_b16 v[134:135], %[w1] offset:56832\n\t" \
    "s_waitcnt lgkmcnt(8)\n\tv_mfma_f32_16x16x32_bf16 v[160:163], v[104:107], v[88:91], v[160:163]\n\tv_mfma_f32_16x16x32_bf16 v[164:167], v[104:107], v[92:95], v[164:167]\n\tv_mfma_f32_16x16x32_bf16 v[168:171], v[104:107], v[96:99], v[168:171]\n\t" \
    "v_mfma_f32_16x16x32_bf16 v[172:175], v[104:107], v[100:103], v[172:175]\n\tv_mfma_f32_16x16x32_bf16 v[176:179], v[108:111], v[88:91], v[176:179]\n\tv_mfma_f32_16x16x32_bf16 v[180:183], v[108:111], v[92:95], v[180:183]\n\tv_mfma_f32_16x16x32_bf16 v[184:187], v[108:111], v[96:99], v[184:187]\n\t" \
    "v_mfma_f32_16x16x32_bf16 v[188:191], v[108:111], v[100:103], v[188:191]\n\ts_waitcnt lgkmcnt(0)\n\ts_barrier\n\tv_mfma_f32_16x16x32_bf16 v[160:163], v[128:131], v[112:115], v[160:163]\n\t" \
    "v_mfma_f32_16x16x32_bf16 v[164:167], v[128:131], v[116:119], v[164:167]\n\tv_mfma_f32_16x16x32_bf16 v[168:171], v[128:131], v[120:123], v[168:171]\n\tv_mfma_f32_16x16x32_bf16 v[172:175], v[128:131], v[124:127], v[172:175]\n\tv_mfma_f32_16x16x32_bf16 v[176:179], v[132:135], v[112:115], v[176:179]\n\t" \
    "v_mfma_f32_16x16x32_bf16 v[180:183], v[132:135], v[116:119], v[180:183]\n\tv_mfma_f32_16x16x32_bf16 v[184:187], v[132:135], v[120:123], v[184:187]\n\tv_mfma_f32_16x16x32_bf16 v[188:191], v[132:135], v[124:127], v[188:191]\n\ts_nop 7\n\t"
#define LOADER_A \
    "1:\n\ts_mul_i32 %[dl], %[xo], 0x600\n\ts_add_u32 s40, %[xb], %[xo]\n\ts_addc_u32 s41, %[xh], 0\n\t" \
    "s_mov_b32 m0, %[dx]\n\ts_add_u32 s42, %[wb], %[dl]\n\ts_addc_u32 s43, %[wh], 0\n\tglobal_load_lds_dwordx4 %[x0], s[40:41]\n\t" \
    "global_load_lds_dwordx4 %[x1], s[40:41] offset:1024\n\tglobal_load_lds_dwordx4 %[x2], s[40:41] offset:2048\n\tglobal_load_lds_dwordx4 %[x3], s[40:41] offset:3072\n\ts_add_u32 m0, %[dx], %[dw]\n\t" \
    "s_nop 0\n\tglobal_load_lds_dwordx4 %[w0], s[42:43]\n\tglobal_load_lds_dwordx4 %[w1], s[42:43] offset:1024\n\tglobal_load_lds_dwordx4 %[w2], s[42:43] offset:2048\n\t" \
    "global_load_lds_dwordx4 %[w3], s[42:43] offset:3072\n\ts_add_u32 m0, m0, 0x1000\n\ts_nop 0\n\tglobal_load_lds_dwordx4 %[w4], s[42:43]\n\t" \
    "global_load_lds_dwordx4 %[w5], s[42:43] offset:1024\n\ts_add_u32 m0, %[dx], %[dh]\n\ts_mov_b32 exec_hi, 0\n\tglobal_load_lds_dwordx4 %[ho], s[40:41]\n\t" \
    "s_mov_b32 exec_hi, -1\n\ts_add_u32 %[xo], %[xo], 0x80\n\ts_and_b32 %[xo], %[xo], 0x3ff\n\ts_cmp_eq_u32 %[pi], 2\n\t" \
    "s_cselect_b32 %[dl], %[neg], %[pos]\n\ts_cselect_b32 %[pi], -1, %[pi]\n\ts_add_u32 %[pi], %[pi], 1\n\ts_add_u32 %[dx], %[dx], %[dl]\n\t" \
    "s_cmp_lg_u32 %[pi], 0\n\ts_cbranch_scc1 1b\n\ts_waitcnt vmcnt(22) lgkmcnt(0)\n\ts_barrier\n\t" \
    "2:\n\ts_waitcnt vmcnt(11)\n\ts_barrier\n\ts_mul_i32 %[dl], %[xo], 0x600\n\t" \
    "s_add_u32 s40, %[xb], %[xo]\n\ts_addc_u32 s41, %[xh], 0\n\ts_mov_b32 m0, %[dx]\n\ts_add_u32 s42, %[wb], %[dl]\n\t" \
    "s_addc_u32 s43, %[wh], 0\n\tglobal_load_lds_dwordx4 %[x0], s[40:41]\n\tglobal_load_lds_dwordx4 %[x1], s[40:41] offset:1024\n\tglobal_load_lds_dwordx4 %[x2], s[40:41] offset:2048\n\t" \
    "global_load_lds_dwordx4 %[x3], s[40:41] offset:3072\n\ts_add_u32 m0, %[dx], %[dw]\n\ts_nop 0\n\tglobal_load_lds_dwordx4 %[w0], s[42:43]\n\t" \
    "global_load_lds_dwordx4 %[w1], s[42:43] offset:1024\n\tglobal_load_lds_dwordx4 %[w2], s[42:43] offset:2048\n\tglobal_load_lds_dwordx4 %[w3], s[42:43] offset:3072\n\ts_add_u32 m0, m0, 0x1000\n\t" \
    "s_nop 0\n\tglobal_load_lds_dwordx4 %[w4], s[42:43]\n\tglobal_load_lds_dwordx4 %[w5], s[42:43] offset:1024\n\ts_add_u32 m0, %[dx], %[dh]\n\t" \
    "s_mov_b32 exec_hi, 0\n\tglobal_load_lds_dwordx4 %[ho], s[40:41]\n\ts_mov_b32 exec_hi, -1\n\ts_add_u32 %[xo], %[xo], 0x80\n\t" \
    "s_and_b32 %[xo], %[xo], 0x3ff\n\ts_cmp_eq_u32 %[pi], 2\n\ts_cselect_b32 %[dl], %[neg], %[pos]\n\ts_cselect_b32 %[pi], -1, %[pi]\n\t" \
    "s_add_u32 %[pi], %[pi], 1\n\ts_add_u32 %[dx], %[dx], %[dl]\n\ts_sub_u32 %[cnt], %[cnt], 1\n\ts_cmp_lg_u32 %[cnt], 0\n\t" \
    "s_cbranch_scc1 2b\n\ts_waitcnt vmcnt(11)\n\ts_barrier\n\ts_waitcnt vmcnt(0)\n\t" \
    "s_barrier\n\t"
#define LOADER_B \
    "1:\n\ts_mul_i32 %[dl], %[xo], 0x600\n\ts_add_u32 s40, %[xb], %[xo]\n\ts_addc_u32 s41, %[xh], 0\n\t" \
    "s_mov_b32 m0, %[dx]\n\ts_add_u32 s42, %[wb], %[dl]\n\ts_addc_u32 s43, %[wh], 0\n\tglobal_load_lds_dwordx4 %[x0], s[40:41]\n\t" \
    "global_load_lds_dwordx4 %[x1], s[40:41] offset:1024\n\tglobal_load_lds_dwordx4 %[x2], s[40:41] offset:2048\n\tglobal_load_lds_dwordx4 %[x3], s[40:41] offset:3072\n\ts_add_u32 m0, %[dx], %[dw]\n\t" \
    "s_nop 0\n\tglobal_load_lds_dwordx4 %[w0], s[42:43]\n\tglobal_load_lds_dwordx4 %[w1], s[42:43] offset:1024\n\tglobal_load_lds_dwordx4 %[w2], s[42:43] offset:2048\n\t" \
    "global_load_lds_dwordx4 %[w3], s[42:43] offset:3072\n\ts_add_u32 m0, m0, 0x1000\n\ts_nop 0\n\tglobal_load_lds_dwordx4 %[w4], s[42:43]\n\t" \
    "global_load_lds_dwordx4 %[w5], s[42:43] offset:1024\n\ts_add_u32 %[xo], %[xo], 0x80\n\ts_and_b32 %[xo], %[xo], 0x3ff\n\ts_cmp_eq_u32 %[pi], 2\n\t" \
    "s_cselect_b32 %[dl], %[neg], %[pos]\n\ts_cselect_b32 %[pi], -1, %[pi]\n\ts_add_u32 %[pi], %[pi], 1\n\ts_add_u32 %[dx], %[dx], %[dl]\n\t" \
    "s_cmp_lg_u32 %[pi], 0\n\ts_cbranch_scc1 1b\n\ts_waitcnt vmcnt(20) lgkmcnt(0)\n\ts_barrier\n\t" \
    "2:\n\ts_waitcnt vmcnt(10)\n\ts_barrier\n\ts_mul_i32 %[dl], %[xo], 0x600\n\t" \
    "s_add_u32 s40, %[xb], %[xo]\n\ts_addc_u32 s41, %[xh], 0\n\ts_mov_b32 m0, %[dx]\n\ts_add_u32 s42, %[wb], %[dl]\n\t" \
    "s_addc_u32 s43, %[wh], 0\n\tglobal_load_lds_dwordx4 %[x0], s[40:41]\n\tglobal_load_lds_dwordx4 %[x1], s[40:41] offset:1024\n\tglobal_load_lds_dwordx4 %[x2], s[40:41] offset:2048\n\t" \
    "global_load_lds_dwordx4 %[x3], s[40:41] offset:3072\n\ts_add_u32 m0, %[dx], %[dw]\n\ts_nop 0\n\tglobal_load_lds_dwordx4 %[w0], s[42:43]\n\t" \
    "global_load_lds_dwordx4 %[w1], s[42:43] offset:1024\n\tglobal_load_lds_dwordx4 %[w2], s[42:43] offset:2048\n\tglobal_load_lds_dwordx4 %[w3], s[42:43] offset:3072\n\ts_add_u32 m0, m0, 0x1000\n\t" \
    "s_nop 0\n\tglobal_load_lds_dwordx4 %[w4], s[42:43]\n\tglobal_load_lds_dwordx4 %[w5], s[42:43] offset:1024\n\ts_add_u32 %[xo], %[xo], 0x80\n\t" \
    "s_and_b32 %[xo], %[xo], 0x3ff\n\ts_cmp_eq_u32 %[pi], 2\n\ts_cselect_b32 %[dl], %[neg], %[pos]\n\ts_cselect_b32 %[pi], -1, %[pi]\n\t" \
    "s_add_u32 %[pi], %[pi], 1\n\ts_add_u32 %[dx], %[dx], %[dl]\n\ts_sub_u32 %[cnt], %[cnt], 1\n\ts_cmp_lg_u32 %[cnt], 0\n\t" \
    "s_cbranch_scc1 2b\n\ts_waitcnt vmcnt(10)\n\ts_barrier\n\ts_waitcnt vmcnt(0)\n\t" \
    "s_barrier\n\ts_barrier\n\t"
#define PROJ_A \
    "global_load_dwordx2 %[fl], %[fo], %[fp] sc1\n\tds_read_b64_tr_b16 v[128:129], %[p0] offset:0\n\tds_read_b64_tr_b16 v[130:131], %[p0] offset:1024\n\tds_read_b64_tr_b16 v[132:133], %[p1] offset:0\n\t" \
    "ds_read_b64_tr_b16 v[134:135], %[p1] offset:1024\n\tds_read_b64_tr_b16 v[136:137], %[p2] offset:0\n\tds_read_b64_tr_b16 v[138:139], %[p2] offset:1024\n\tds_read_b64_tr_b16 v[140:141], %[p3] offset:0\n\t" \
    "ds_read_b64_tr_b16 v[142:143], %[p3] offset:1024\n\tds_read_b64_tr_b16 v[144:145], %[p0] offset:4096\n\tds_read_b64_tr_b16 v[146:147], %[p0] offset:5120\n\tds_read_b64_tr_b16 v[148:149], %[p1] offset:4096\n\t" \
    "ds_read_b64_tr_b16 v[150:151], %[p1] offset:5120\n\tds_read_b64_tr_b16 v[152:153], %[p2] offset:4096\n\tds_read_b64_tr_b16 v[154:155], %[p2] offset:5120\n\ts_waitcnt lgkmcnt(12)\n\t" \
    "v_mfma_f32_16x16x32_bf16 %[c0], v[128:131], v[64:67], 0\n\tds_read_b64_tr_b16 v[156:157], %[p3] offset:4096\n\tds_read_b64_tr_b16 v[158:159], %[p3] offset:5120\n\ts_waitcnt lgkmcnt(12)\n\t" \
    "v_mfma_f32_16x16x32_bf16 %[c1], v[132:135], v[64:67], 0\n\tds_read_b64_tr_b16 v[128:129], %[p0] offset:8192\n\tds_read_b64_tr_b16 v[130:131], %[p0] offset:9216\n\ts_waitcnt lgkmcnt(12)\n\t" \
    "v_mfma_f32_16x16x32_bf16 %[c2], v[136:139], v[64:67], 0\n\tds_read_b64_tr_b16 v[132:133], %[p1] offset:8192\n\tds_read_b64_tr_b16 v[134:135], %[p1] offset:9216\n\ts_waitcnt lgkmcnt(12)\n\t" \
    "v_mfma_f32_16x16x32_bf16 %[c3], v[140:143], v[64:67], 0\n\tds_read_b64_tr_b16 v[136:137], %[p2] offset:8192\n\tds_read_b64_tr_b16 v[138:139], %[p2] offset:9216\n\ts_waitcnt lgkmcnt(12)\n\t" \
    "v_mfma_f32_16x16x32_bf16 %[c0], v[144:147], v[68:71], %[c0]\n\tds_read_b64_tr_b16 v[140:141], %[p3] offset:8192\n\tds_read_b64_tr_b16 v[142:143], %[p3] offset:9216\n\ts_waitcnt lgkmcnt(12)\n\t" \
    "v_mfma_f32_16x16x32_bf16 %[c1], v[148:151], v[68:71], %[c1]\n\tds_read_b64_tr_b16 v[144:145], %[p0] offset:12288\n\tds_read_b64_tr_b16 v[146:147], %[p0] offset:13312\n\ts_waitcnt lgkmcnt(12)\n\t" \
    "v_mfma_f32_16x16x32_bf16 %[c2], v[152:155], v[68:71], %[c2]\n\tds_read_b64_tr_b16 v[148:149], %[p1] offset:12288\n\tds_read_b64_tr_b16 v[150:151], %[p1] offset:13312\n\ts_waitcnt lgkmcnt(12)\n\t" \
    "v_mfma_f32_16x16x32_bf16 %[c3], v[156:159], v[68:71], %[c3]\n\tds_read_b64_tr_b16 v[152:153], %[p2] offset:12288\n\tds_read_b64_tr_b16 v[154:155], %[p2] offset:13312\n\ts_waitcnt lgkmcnt(12)\n\t" \
    "v_mfma_f32_16x16x32_bf16 %[c0], v[128:131], v[72:75], %[c0]\n\tds_read_b64_tr_b16 v[156:157], %[p3] offset:12288\n\tds_read_b64_tr_b16 v[158:159], %[p3] offset:13312\n\ts_waitcnt lgkmcnt(12)\n\t" \
    "v_mfma_f32_16x16x32_bf16 %[c1], v[132:135], v[72:75], %[c1]\n\tds_read_b64_tr_b16 v[128:129], %[p0] offset:16384\n\tds_read_b64_tr_b16 v[130:131], %[p0] offset:17408\n\ts_waitcnt lgkmcnt(12)\n\t" \
    "v_mfma_f32_16x16x32_bf16 %[c2], v[136:139], v[72:75], %[c2]\n\tds_read_b64_tr_b16 v[132:133], %[p1] offset:16384\n\tds_read_b64_tr_b16 v[134:135], %[p1] offset:17408\n\ts_waitcnt lgkmcnt(12)\n\t" \
    "v_mfma_f32_16x16x32_bf16 %[c3], v[140:143], v[72:75], %[c3]\n\tds_read_b64_tr_b16 v[136:137], %[p2] offset:16384\n\tds_read_b64_tr_b16 v[138:139], %[p2] offset:17408\n\ts_waitcnt lgkmcnt(12)\n\t" \
    "v_mfma_f32_16x16x32_bf16 %[c0], v[144:147], v[76:79], %[c0]\n\tds_read_b64_tr_b16 v[140:141], %[p3] offset:16384\n\tds_read_b64_tr_b16 v[142:143], %[p3] offset:17408\n\ts_waitcnt lgkmcnt(12)\n\t" \
    "v_mfma_f32_16x16x32_bf16 %[c1], v[148:151], v[76:79], %[c1]\n\tds_read_b64_tr_b16 v[144:145], %[p0] offset:20480\n\tds_read_b64_tr_b16 v[146:147], %[p0] offset:21504\n\ts_waitcnt lgkmcnt(12)\n\t" \
    "v_mfma_f32_16x16x32_bf16 %[c2], v[152:155], v[76:79], %[c2]\n\tds_read_b64_tr_b16 v[148:149], %[p1] offset:20480\n\tds_read_b64_tr_b16 v[150:151], %[p1] offset:21504\n\ts_waitcnt lgkmcnt(12)\n\t" \
    "v_mfma_f32_16x16x32_bf16 %[c3], v[156:159], v[76:79], %[c3]\n\tds_read_b64_tr_b16 v[152:153], %[p2] offset:20480\n\tds_read_b64_tr_b16 v[154:155], %[p2] offset:21504\n\ts_waitcnt lgkmcnt(12)\n\t" \
    "v_mfma_f32_16x16x32_bf16 %[c0], v[128:131], v[80:83], %[c0]\n\tds_read_b64_tr_b16 v[156:157], %[p3] offset:20480\n\tds_read_b64_tr_b16 v[158:159], %[p3] offset:21504\n\ts_waitcnt lgkmcnt(12)\n\t" \
    "v_mfma_f32_16x16x32_bf16 %[c1], v[132:135], v[80:83], %[c1]\n\tds_read_b64_tr_b16 v[128:129], %[p0] offset:24576\n\tds_read_b64_tr_b16 v[130:131], %[p0] offset:25600\n\ts_waitcnt lgkmcnt(12)\n\t" \
    "v_mfma_f32_16x16x32_bf16 %[c2], v[136:139], v[80:83], %[c2]\n\tds_read_b64_tr_b16 v[132:133], %[p1] offset:24576\n\tds_read_b64_tr_b16 v[134:135], %[p1] offset:25600\n\ts_waitcnt lgkmcnt(12)\n\t" \
    "v_mfma_f32_16x16x32_bf16 %[c3], v[140:143], v[80:83], %[c3]\n\tds_read_b64_tr_b16 v[136:137], %[p2] offset:24576\n\tds_read_b64_tr_b16 v[138:139], %[p2] offset:25600\n\ts_waitcnt lgkmcnt(12)\n\t" \
    "v_mfma_f32_16x16x32_bf16 %[c0], v[144:147], v[84:87], %[c0]\n\tds_read_b64_tr_b16 v[140:141], %[p3] offset:24576\n\tds_read_b64_tr_b16 v[142:143], %[p3] offset:25600\n\ts_waitcnt lgkmcnt(12)\n\t" \
    "v_mfma_f32_16x16x32_bf16 %[c1], v[148:151], v[84:87], %[c1]\n\tds_read_b64_tr_b16 v[144:145], %[p0] offset:28672\n\tds_read_b64_tr_b16 v[146:147], %[p0] offset:29696\n\ts_waitcnt lgkmcnt(12)\n\t" \
    "v_mfma_f32_16x16x32_bf16 %[c2], v[152:155], v[84:87], %[c2]\n\tds_read_b64_tr_b16 v[148:149], %[p1] offset:28672\n\tds_read_b64_tr_b16 v[150:151], %[p1] offset:29696\n\ts_waitcnt lgkmcnt(12)\n\t" \
    "v_mfma_f32_16x16x32_bf16 %[c3], v[156:159], v[84:87], %[c3]\n\tds_read_b64_tr_b16 v[152:153], %[p2] offset:28672\n\tds_read_b64_tr_b16 v[154:155], %[p2] offset:29696\n\ts_waitcnt lgkmcnt(12)\n\t" \
    "v_mfma_f32_16x16x32_bf16 %[c0], v[128:131], v[88:91], %[c0]\n\tds_read_b64_tr_b16 v[156:157], %[p3] offset:28672\n\tds_read_b64_tr_b16 v[158:159], %[p3] offset:29696\n\ts_waitcnt lgkmcnt(12)\n\t" \
    "v_mfma_f32_16x16x32_bf16 %[c1], v[132:135], v[88:91], %[c1]\n\tds_read_b64_tr_b16 v[128:129], %[p0] offset:32768\n\tds_read_b64_tr_b16 v[130:131], %[p0] offset:33792\n\ts_waitcnt lgkmcnt(12)\n\t" \
    "v_mfma_f32_16x16x32_bf16 %[c2], v[136:139], v[88:91], %[c2]\n\tds_read_b64_tr_b16 v[132:133], %[p1] offset:32768\n\tds_read_b64_tr_b16 v[134:135], %[p1] offset:33792\n\ts_waitcnt lgkmcnt(12)\n\t" \
    "v_mfma_f32_16x16x32_bf16 %[c3], v[140:143], v[88:91], %[c3]\n\tds_read_b64_tr_b16 v[136:137], %[p2] offset:32768\n\tds_read_b64_tr_b16 v[138:139], %[p2] offset:33792\n\ts_waitcnt lgkmcnt(12)\n\t" \
    "v_mfma_f32_16x16x32_bf16 %[c0], v[144:147], v[92:95], %[c0]\n\tds_read_b64_tr_b16 v[140:141], %[p3] offset:32768\n\tds_read_b64_tr_b16 v[142:143], %[p3] offset:33792\n\ts_waitcnt lgkmcnt(12)\n\t" \
    "v_mfma_f32_16x16x32_bf16 %[c1], v[148:151], v[92:95], %[c1]\n\tds_read_b64_tr_b16 v[144:145], %[p0] offset:36864\n\tds_read_b64_tr_b16 v[146:147], %[p0] offset:37888\n\ts_waitcnt lgkmcnt(12)\n\t" \
    "v_mfma_f32_16x16x32_bf16 %[c2], v[152:155], v[92:95], %[c2]\n\tds_read_b64_tr_b16 v[148:149], %[p1] offset:36864\n\tds_read_b64_tr_b16 v[150:151], %[p1] offset:37888\n\ts_waitcnt lgkmcnt(12)\n\t" \
    "v_mfma_f32_16x16x32_bf16 %[c3], v[156:159], v[92:95], %[c3]\n\tds_read_b64_tr_b16 v[152:153], %[p2] offset:36864\n\tds_read_b64_tr_b16 v[154:155], %[p2] offset:37888\n\ts_waitcnt lgkmcnt(12)\n\t" \
    "v_mfma_f32_16x16x32_bf16 %[c0], v[128:131], v[96:99], %[c0]\n\tds_read_b64_tr_b16 v[156:157], %[p3] offset:36864\n\tds_read_b64_tr_b16 v[158:159], %[p3] offset:37888\n\ts_waitcnt lgkmcnt(12)\n\t" \
    "v_mfma_f32_16x16x32_bf16 %[c1], v[132:135], v[96:99], %[c1]\n\tds_read_b64_tr_b16 v[128:129], %[p0] offset:40960\n\tds_read_b64_tr_b16 v[130:131], %[p0] offset:41984\n\ts_waitcnt lgkmcnt(12)\n\t" \
    "v_mfma_f32_16x16x32_bf16 %[c2], v[136:139], v[96:99], %[c2]\n\tds_read_b64_tr_b16 v[132:133], %[p1] offset:40960\n\tds_read_b64_tr_b16 v[134:135], %[p1] offset:41984\n\ts_waitcnt lgkmcnt(12)\n\t" \
    "v_mfma_f32_16x16x32_bf16 %[c3], v[140:143], v[96:99], %[c3]\n\tds_read_b64_tr_b16 v[136:137], %[p2] offset:40960\n\tds_read_b64_tr_b16 v[138:139], %[p2] offset:41984\n\ts_waitcnt lgkmcnt(12)\n\t" \
    "v_mfma_f32_16x16x32_bf16 %[c0], v[144:147], v[100:103], %[c0]\n\tds_read_b64_tr_b16 v[140:141], %[p3] offset:40960\n\tds_read_b64_tr_b16 v[142:143], %[p3] offset:41984\n\ts_waitcnt lgkmcnt(12)\n\t" \
    "v_mfma_f32_16x16x32_bf16 %[c1], v[148:151], v[100:103], %[c1]\n\tds_read_b64_tr_b16 v[144:145], %[p0] offset:45056\n\tds_read_b64_tr_b16 v[146:147], %[p0] offset:46080\n\ts_waitcnt lgkmcnt(12)\n\t" \
    "v_mfma_f32_16x16x32_bf16 %[c2], v[152:155], v[100:103], %[c2]\n\tds_read_b64_tr_b16 v[148:149], %[p1] offset:45056\n\tds_read_b64_tr_b16 v[150:151], %[p1] offset:46080\n\ts_waitcnt lgkmcnt(12)\n\t" \
    "v_mfma_f32_16x16x32_bf16 %[c3], v[156:159], v[100:103], %[c3]\n\tds_read_b64_tr_b16 v[152:153], %[p2] offset:45056\n\tds_read_b64_tr_b16 v[154:155], %[p2] offset:46080\n\ts_waitcnt lgkmcnt(12)\n\t" \
    "v_mfma_f32_16x16x32_bf16 %[c0], v[128:131], v[104:107], %[c0]\n\tds_read_b64_tr_b16 v[156:157], %[p3] offset:45056\n\tds_read_b64_tr_b16 v[158:159], %[p3] offset:46080\n\ts_waitcnt lgkmcnt(12)\n\t" \
    "v_mfma_f32_16x16x32_bf16 %[c1], v[132:135], v[104:107], %[c1]\n\tds_read_b64_tr_b16 v[128:129], %[p0] offset:49152\n\tds_read_b64_tr_b16 v[130:131], %[p0] offset:50176\n\ts_waitcnt lgkmcnt(12)\n\t" \
    "v_mfma_f32_16x16x32_bf16 %[c2], v[136:139], v[104:107], %[c2]\n\tds_read_b64_tr_b16 v[132:133], %[p1] offset:49152\n\tds_read_b64_tr_b16 v[134:135], %[p1] offset:50176\n\ts_waitcnt lgkmcnt(12)\n\t" \
    "v_mfma_f32_16x16x32_bf16 %[c3], v[140:143], v[104:107], %[c3]\n\tds_read_b64_tr_b16 v[136:137], %[p2] offset:49152\n\tds_read_b64_tr_b16 v[138:139], %[p2] offset:50176\n\ts_waitcnt lgkmcnt(12)\n\t" \
    "v_mfma_f32_16x16x32_bf16 %[c0], v[144:147], v[108:111], %[c0]\n\tds_read_b64_tr_b16 v[140:141], %[p3] offset:49152\n\tds_read_b64_tr_b16 v[142:143], %[p3] offset:50176\n\ts_waitcnt lgkmcnt(12)\n\t" \
    "v_mfma_f32_16x16x32_bf16 %[c1], v[148:151], v[108:111], %[c1]\n\tds_read_b64_tr_b16 v[144:145], %[p0] offset:53248\n\tds_read_b64_tr_b16 v[146:147], %[p0] offset:54272\n\ts_waitcnt lgkmcnt(12)\n\t" \
    "v_mfma_f32_16x16x32_bf16 %[c2], v[152:155], v[108:111], %[c2]\n\tds_read_b64_tr_b16 v[148:149], %[p1] offset:53248\n\tds_read_b64_tr_b16 v[150:151], %[p1] offset:54272\n\ts_waitcnt lgkmcnt(12)\n\t" \
    "v_mfma_f32_16x16x32_bf16 %[c3], v[156:159], v[108:111], %[c3]\n\tds_read_b64_tr_b16 v[152:153], %[p2] offset:53248\n\tds_read_b64_tr_b16 v[154:155], %[p2] offset:54272\n\ts_waitcnt lgkmcnt(12)\n\t" \
    "v_mfma_f32_16x16x32_bf16 %[c0], v[128:131], v[112:115], %[c0]\n\tds_read_b64_tr_b16 v[156:157], %[p3] offset:53248\n\tds_read_b64_tr_b16 v[158:159], %[p3] offset:54272\n\ts_waitcnt lgkmcnt(12)\n\t" \
    "v_mfma_f32_16x16x32_bf16 %[c1], v[132:135], v[112:115], %[c1]\n\tds_read_b64_tr_b16 v[128:129], %[p0] offset:57344\n\tds_read_b64_tr_b16 v[130:131], %[p0] offset:58368\n\ts_waitcnt lgkmcnt(12)\n\t" \
    "v_mfma_f32_16x16x32_bf16 %[c2], v[136:139], v[112:115], %[c2]\n\tds_read_b64_tr_b16 v[132:133], %[p1] offset:57344\n\tds_read_b64_tr_b16 v[134:135], %[p1] offset:58368\n\ts_waitcnt lgkmcnt(12)\n\t" \
    "v_mfma_f32_16x16x32_bf16 %[c3], v[140:143], v[112:115], %[c3]\n\tds_read_b64_tr_b16 v[136:137], %[p2] offset:57344\n\tds_read_b64_tr_b16 v[138:139], %[p2] offset:58368\n\ts_waitcnt lgkmcnt(12)\n\t" \
    "v_mfma_f32_16x16x32_bf16 %[c0], v[144:147], v[116:119], %[c0]\n\tds_read_b64_tr_b16 v[140:141], %[p3] offset:57344\n\tds_read_b64_tr_b16 v[142:143], %[p3] offset:58368\n\ts_waitcnt lgkmcnt(12)\n\t" \
    "v_mfma_f32_16x16x32_bf16 %[c1], v[148:151], v[116:119], %[c1]\n\tds_read_b64_tr_b16 v[144:145], %[p0] offset:61440\n\tds_read_b64_tr_b16 v[146:147], %[p0] offset:62464\n\ts_waitcnt lgkmcnt(12)\n\t" \
    "v_mfma_f32_16x16x32_bf16 %[c2], v[152:155], v[116:119], %[c2]\n\tds_read_b64_tr_b16 v[148:149], %[p1] offset:61440\n\tds_read_b64_tr_b16 v[150:151], %[p1] offset:62464\n\ts_waitcnt lgkmcnt(12)\n\t" \
    "v_mfma_f32_16x16x32_bf16 %[c3], v[156:159], v[116:119], %[c3]\n\tds_read_b64_tr_b16 v[152:153], %[p2] offset:61440\n\tds_read_b64_tr_b16 v[154:155], %[p2] offset:62464\n\ts_waitcnt lgkmcnt(12)\n\t" \
    "v_mfma_f32_16x16x32_bf16 %[c0], v[128:131], v[120:123], %[c0]\n\tds_read_b64_tr_b16 v[156:157], %[p3] offset:61440\n\tds_read_b64_tr_b16 v[158:159], %[p3] offset:62464\n\ts_waitcnt lgkmcnt(12)\n\t" \
    "v_mfma_f32_16x16x32_bf16 %[c1], v[132:135], v[120:123], %[c1]\n\ts_waitcnt lgkmcnt(10)\n\tv_mfma_f32_16x16x32_bf16 %[c2], v[136:139], v[120:123], %[c2]\n\ts_waitcnt lgkmcnt(8)\n\t" \
    "v_mfma_f32_16x16x32_bf16 %[c3], v[140:143], v[120:123], %[c3]\n\ts_waitcnt lgkmcnt(6)\n\tv_mfma_f32_16x16x32_bf16 %[c0], v[144:147], v[124:127], %[c0]\n\ts_waitcnt lgkmcnt(4)\n\t" \
    "v_mfma_f32_16x16x32_bf16 %[c1], v[148:151], v[124:127], %[c1]\n\ts_waitcnt lgkmcnt(2)\n\tv_mfma_f32_16x16x32_bf16 %[c2], v[152:155], v[124:127], %[c2]\n\ts_waitcnt lgkmcnt(0)\n\t" \
    "v_mfma_f32_16x16x32_bf16 %[c3], v[156:159], v[124:127], %[c3]\n\ts_waitcnt vmcnt(0)\n\ts_nop 6\n\t"
#define ROWS_LD \
    "global_load_dwordx4 v[64:67], %[of0], %[o0] sc1\n\tglobal_load_dwordx4 v[68:71], %[of0], %[o0] offset:2048 sc1\n\tglobal_load_dwordx4 v[72:75], %[of1], %[o0] sc1\n\tglobal_load_dwordx4 v[76:79], %[of1], %[o0] offset:2048 sc1\n\t" \
    "global_load_dwordx4 v[80:83], %[of2], %[o0] sc1\n\tglobal_load_dwordx4 v[84:87], %[of2], %[o0] offset:2048 sc1\n\tglobal_load_dwordx4 v[88:91], %[of3], %[o0] sc1\n\tglobal_load_dwordx4 v[92:95], %[of3], %[o0] offset:2048 sc1\n\t" \
    "global_load_dwordx4 v[96:99], %[of0], %[o1] sc1\n\tglobal_load_dwordx4 v[100:103], %[of0], %[o1] offset:2048 sc1\n\tglobal_load_dwordx4 v[104:107], %[of1], %[o1] sc1\n\tglobal_load_dwordx4 v[108:111], %[of1], %[o1] offset:2048 sc1\n\t" \
    "global_load_dwordx4 v[112:115], %[of2], %[o1] sc1\n\tglobal_load_dwordx4 v[116:119], %[of2], %[o1] offset:2048 sc1\n\tglobal_load_dwordx4 v[120:123], %[of3], %[o1] sc1\n\tglobal_load_dwordx4 v[124:127], %[of3], %[o1] offset:2048 sc1\n\t"
#define PROJ_B_W \
    "ds_read_b64_tr_b16 v[128:129], %[p0] offset:0\n\tds_read_b64_tr_b16 v[130:131], %[p0] offset:1024\n\tds_read_b64_tr_b16 v[132:133], %[p1] offset:0\n\tds_read_b64_tr_b16 v[134:135], %[p1] offset:1024\n\t" \
    "ds_read_b64_tr_b16 v[136:137], %[p2] offset:0\n\tds_read_b64_tr_b16 v[138:139], %[p2] offset:1024\n\tds_read_b64_tr_b16 v[140:141], %[p3] offset:0\n\tds_read_b64_tr_b16 v[142:143], %[p3] offset:1024\n\t" \
    "ds_read_b64_tr_b16 v[144:145], %[p0] offset:4096\n\tds_read_b64_tr_b16 v[146:147], %[p0] offset:5120\n\tds_read_b64_tr_b16 v[148:149], %[p1] offset:4096\n\tds_read_b64_tr_b16 v[150:151], %[p1] offset:5120\n\t" \
    "ds_read_b64_tr_b16 v[152:153], %[p2] offset:4096\n\tds_read_b64_tr_b16 v[154:155], %[p2] offset:5120\n\tds_read_b64_tr_b16 v[156:157], %[p3] offset:4096\n\tds_read_b64_tr_b16 v[158:159], %[p3] offset:5120\n\t" \
    "ds_read_b64_tr_b16 v[160:161], %[p0] offset:8192\n\tds_read_b64_tr_b16 v[162:163], %[p0] offset:9216\n\tds_read_b64_tr_b16 v[164:165], %[p1] offset:8192\n\tds_read_b64_tr_b16 v[166:167], %[p1] offset:9216\n\t" \
    "ds_read_b64_tr_b16 v[168:169], %[p2] offset:8192\n\tds_read_b64_tr_b16 v[170:171], %[p2] offset:9216\n\tds_read_b64_tr_b16 v[172:173], %[p3] offset:8192\n\tds_read_b64_tr_b16 v[174:175], %[p3] offset:9216\n\t" \
    "ds_read_b64_tr_b16 v[176:177], %[p0] offset:12288\n\tds_read_b64_tr_b16 v[178:179], %[p0] offset:13312\n\tds_read_b64_tr_b16 v[180:181], %[p1] offset:12288\n\tds_read_b64_tr_b16 v[182:183], %[p1] offset:13312\n\t" \
    "ds_read_b64_tr_b16 v[184:185], %[p2] offset:12288\n\tds_read_b64_tr_b16 v[186:187], %[p2] offset:13312\n\tds_read_b64_tr_b16 v[188:189], %[p3] offset:12288\n\tds_read_b64_tr_b16 v[190:191], %[p3] offset:13312\n\t" \
    "ds_read_b64_tr_b16 v[192:193], %[p0] offset:16384\n\tds_read_b64_tr_b16 v[194:195], %[p0] offset:17408\n\tds_read_b64_tr_b16 v[196:197], %[p1] offset:16384\n\tds_read_b64_tr_b16 v[198:199], %[p1] offset:17408\n\t" \
    "ds_read_b64_tr_b16 v[200:201], %[p2] offset:16384\n\tds_read_b64_tr_b16 v[202:203], %[p2] offset:17408\n\tds_read_b64_tr_b16 v[204:205], %[p3] offset:16384\n\tds_read_b64_tr_b16 v[206:207], %[p3] offset:17408\n\t" \
    "ds_read_b64_tr_b16 v[208:209], %[p0] offset:20480\n\tds_read_b64_tr_b16 v[210:211], %[p0] offset:21504\n\tds_read_b64_tr_b16 v[212:213], %[p1] offset:20480\n\tds_read_b64_tr_b16 v[214:215], %[p1] offset:21504\n\t" \
    "ds_read_b64_tr_b16 v[216:217], %[p2] offset:20480\n\tds_read_b64_tr_b16 v[218:219], %[p2] offset:21504\n\tds_read_b64_tr_b16 v[220:221], %[p3] offset:20480\n\tds_read_b64_tr_b16 v[222:223], %[p3] offset:21504\n\t" \
    "ds_read_b64_tr_b16 v[224:225], %[p0] offset:24576\n\tds_read_b64_tr_b16 v[226:227], %[p0] offset:25600\n\tds_read_b64_tr_b16 v[228:229], %[p1] offset:24576\n\tds_read_b64_tr_b16 v[230:231], %[p1] offset:25600\n\t" \
    "ds_read_b64_tr_b16 v[232:233], %[p2] offset:24576\n\tds_read_b64_tr_b16 v[234:235], %[p2] offset:25600\n\tds_read_b64_tr_b16 v[236:237], %[p3] offset:24576\n\tds_read_b64_tr_b16 v[238:239], %[p3] offset:25600\n\t" \
    "ds_read_b64_tr_b16 v[240:241], %[p0] offset:28672\n\tds_read_b64_tr_b16 v[242:243], %[p0] offset:29696\n\tds_read_b64_tr_b16 v[244:245], %[p1] offset:28672\n\tds_read_b64_tr_b16 v[246:247], %[p1] offset:29696\n\t" \
    "ds_read_b64_tr_b16 v[248:249], %[p2] offset:28672\n\tds_read_b64_tr_b16 v[250:251], %[p2] offset:29696\n\tds_read_b64_tr_b16 v[252:253], %[p3] offset:28672\n\tds_read_b64_tr_b16 v[254:255], %[p3] offset:29696\n\t" \
    "s_waitcnt lgkmcnt(0)\n\t"
#define PROJ_B \
    "s_waitcnt vmcnt(15)\n\tv_mfma_f32_16x16x32_bf16 %[c0], v[128:131], v[64:67], 0\n\tv_mfma_f32_16x16x32_bf16 %[c1], v[132:135], v[64:67], 0\n\tds_read_b64_tr_b16 v[128:129], %[p0] offset:0\n\t" \
    "ds_read_b64_tr_b16 v[130:131], %[p0] offset:1024\n\tv_mfma_f32_16x16x32_bf16 %[c2], v[136:139], v[64:67], 0\n\tds_read_b64_tr_b16 v[132:133], %[p1] offset:0\n\tds_read_b64_tr_b16 v[134:135], %[p1] offset:1024\n\t" \
    "v_mfma_f32_16x16x32_bf16 %[c3], v[140:143], v[64:67], 0\n\tds_read_b64_tr_b16 v[136:137], %[p2] offset:0\n\tds_read_b64_tr_b16 v[138:139], %[p2] offset:1024\n\ts_waitcnt vmcnt(14)\n\t" \
    "v_mfma_f32_16x16x32_bf16 %[c0], v[144:147], v[68:71], %[c0]\n\tds_read_b64_tr_b16 v[140:141], %[p3] offset:0\n\tds_read_b64_tr_b16 v[142:143], %[p3] offset:1024\n\tv_mfma_f32_16x16x32_bf16 %[c1], v[148:151], v[68:71], %[c1]\n\t" \
    "ds_read_b64_tr_b16 v[144:145], %[p0] offset:4096\n\tds_read_b64_tr_b16 v[146:147], %[p0] offset:5120\n\tv_mfma_f32_16x16x32_bf16 %[c2], v[152:155], v[68:71], %[c2]\n\tds_read_b64_tr_b16 v[148:149], %[p1] offset:4096\n\t" \
    "ds_read_b64_tr_b16 v[150:151], %[p1] offset:5120\n\tv_mfma_f32_16x16x32_bf16 %[c3], v[156:159], v[68:71], %[c3]\n\tds_read_b64_tr_b16 v[152:153], %[p2] offset:4096\n\tds_read_b64_tr_b16 v[154:155], %[p2] offset:5120\n\t" \
    "s_waitcnt vmcnt(13)\n\tv_mfma_f32_16x16x32_bf16 %[c0], v[160:163], v[72:75], %[c0]\n\tds_read_b64_tr_b16 v[156:157], %[p3] offset:4096\n\tds_read_b64_tr_b16 v[158:159], %[p3] offset:5120\n\t" \
    "v_mfma_f32_16x16x32_bf16 %[c1], v[164:167], v[72:75], %[c1]\n\tds_read_b64_tr_b16 v[160:161], %[p0] offset:8192\n\tds_read_b64_tr_b16 v[162:163], %[p0] offset:9216\n\tv_mfma_f32_16x16x32_bf16 %[c2], v[168:171], v[72:75], %[c2]\n\t" \
    "ds_read_b64_tr_b16 v[164:165], %[p1] offset:8192\n\tds_read_b64_tr_b16 v[166:167], %[p1] offset:9216\n\tv_mfma_f32_16x16x32_bf16 %[c3], v[172:175], v[72:75], %[c3]\n\tds_read_b64_tr_b16 v[168:169], %[p2] offset:8192\n\t" \
    "ds_read_b64_tr_b16 v[170:171], %[p2] offset:9216\n\ts_waitcnt vmcnt(12)\n\tv_mfma_f32_16x16x32_bf16 %[c0], v[176:179], v[76:79], %[c0]\n\tds_read_b64_tr_b16 v[172:173], %[p3] offset:8192\n\t" \
    "ds_read_b64_tr_b16 v[174:175], %[p3] offset:9216\n\tv_mfma_f32_16x16x32_bf16 %[c1], v[180:183], v[76:79], %[c1]\n\tds_read_b64_tr_b16 v[176:177], %[p0] offset:12288\n\tds_read_b64_tr_b16 v[178:179], %[p0] offset:13312\n\t" \
    "v_mfma_f32_16x16x32_bf16 %[c2], v[184:187], v[76:79], %[c2]\n\tds_read_b64_tr_b16 v[180:181], %[p1] offset:12288\n\tds_read_b64_tr_b16 v[182:183], %[p1] offset:13312\n\tv_mfma_f32_16x16x32_bf16 %[c3], v[188:191], v[76:79], %[c3]\n\t" \
    "ds_read_b64_tr_b16 v[184:185], %[p2] offset:12288\n\tds_read_b64_tr_b16 v[186:187], %[p2] offset:13312\n\ts_waitcnt vmcnt(11)\n\tv_mfma_f32_16x16x32_bf16 %[c0], v[192:195], v[80:83], %[c0]\n\t" \
    "ds_read_b64_tr_b16 v[188:189], %[p3] offset:12288\n\tds_read_b64_tr_b16 v[190:191], %[p3] offset:13312\n\tv_mfma_f32_16x16x32_bf16 %[c1], v[196:199], v[80:83], %[c1]\n\tds_read_b64_tr_b16 v[192:193], %[p0] offset:16384\n\t" \
    "ds_read_b64_tr_b16 v[194:195], %[p0] offset:17408\n\tv_mfma_f32_16x16x32_bf16 %[c2], v[200:203], v[80:83], %[c2]\n\tds_read_b64_tr_b16 v[196:197], %[p1] offset:16384\n\tds_read_b64_tr_b16 v[198:199], %[p1] offset:17408\n\t" \
    "v_mfma_f32_16x16x32_bf16 %[c3], v[204:207], v[80:83], %[c3]\n\tds_read_b64_tr_b16 v[200:201], %[p2] offset:16384\n\tds_read_b64_tr_b16 v[202:203], %[p2] offset:17408\n\ts_waitcnt vmcnt(10)\n\t" \
    "v_mfma_f32_16x16x32_bf16 %[c0], v[208:211], v[84:87], %[c0]\n\tds_read_b64_tr_b16 v[204:205], %[p3] offset:16384\n\tds_read_b64_tr_b16 v[206:207], %[p3] offset:17408\n\tv_mfma_f32_16x16x32_bf16 %[c1], v[212:215], v[84:87], %[c1]\n\t" \
    "ds_read_b64_tr_b16 v[208:209], %[p0] offset:20480\n\tds_read_b64_tr_b16 v[210:211], %[p0] offset:21504\n\tv_mfma_f32_16x16x32_bf16 %[c2], v[216:219], v[84:87], %[c2]\n\tds_read_b64_tr_b16 v[212:213], %[p1] offset:20480\n\t" \
    "ds_read_b64_tr_b16 v[214:215], %[p1] offset:21504\n\tv_mfma_f32_16x16x32_bf16 %[c3], v[220:223], v[84:87], %[c3]\n\tds_read_b64_tr_b16 v[216:217], %[p2] offset:20480\n\tds_read_b64_tr_b16 v[218:219], %[p2] offset:21504\n\t" \
    "s_waitcnt vmcnt(9)\n\tv_mfma_f32_16x16x32_bf16 %[c0], v[224:227], v[88:91], %[c0]\n\tds_read_b64_tr_b16 v[220:221], %[p3] offset:20480\n\tds_read_b64_tr_b16 v[222:223], %[p3] offset:21504\n\t" \
    "v_mfma_f32_16x16x32_bf16 %[c1], v[228:231], v[88:91], %[c1]\n\tds_read_b64_tr_b16 v[224:225], %[p0] offset:24576\n\tds_read_b64_tr_b16 v[226:227], %[p0] offset:25600\n\tv_mfma_f32_16x16x32_bf16 %[c2], v[232:235], v[88:91], %[c2]\n\t" \
    "ds_read_b64_tr_b16 v[228:229], %[p1] offset:24576\n\tds_read_b64_tr_b16 v[230:231], %[p1] offset:25600\n\tv_mfma_f32_16x16x32_bf16 %[c3], v[236:239], v[88:91], %[c3]\n\tds_read_b64_tr_b16 v[232:233], %[p2] offset:24576\n\t" \
    "ds_read_b64_tr_b16 v[234:235], %[p2] offset:25600\n\ts_waitcnt vmcnt(8)\n\tv_mfma_f32_16x16x32_bf16 %[c0], v[240:243], v[92:95], %[c0]\n\tds_read_b64_tr_b16 v[236:237], %[p3] offset:24576\n\t" \
    "ds_read_b64_tr_b16 v[238:239], %[p3] offset:25600\n\tv_mfma_f32_16x16x32_bf16 %[c1], v[244:247], v[92:95], %[c1]\n\tds_read_b64_tr_b16 v[240:241], %[p0] offset:28672\n\tds_read_b64_tr_b16 v[242:243], %[p0] offset:29696\n\t" \
    "v_mfma_f32_16x16x32_bf16 %[c2], v[248:251], v[92:95], %[c2]\n\tds_read_b64_tr_b16 v[244:245], %[p1] offset:28672\n\tds_read_b64_tr_b16 v[246:247], %[p1] offset:29696\n\tv_mfma_f32_16x16x32_bf16 %[c3], v[252:255], v[92:95], %[c3]\n\t" \
    "ds_read_b64_tr_b16 v[248:249], %[p2] offset:28672\n\tds_read_b64_tr_b16 v[250:251], %[p2] offset:29696\n\ts_waitcnt vmcnt(7) lgkmcnt(15)\n\tv_mfma_f32_16x16x32_bf16 %[c0], v[128:131], v[96:99], %[c0]\n\t" \
    "ds_read_b64_tr_b16 v[252:253], %[p3] offset:28672\n\tds_read_b64_tr_b16 v[254:255], %[p3] offset:29696\n\tv_mfma_f32_16x16x32_bf16 %[c1], v[132:135], v[96:99], %[c1]\n\tv_mfma_f32_16x16x32_bf16 %[c2], v[136:139], v[96:99], %[c2]\n\t" \
    "v_mfma_f32_16x16x32_bf16 %[c3], v[140:143], v[96:99], %[c3]\n\ts_waitcnt vmcnt(6) lgkmcnt(15)\n\tv_mfma_f32_16x16x32_bf16 %[c0], v[144:147], v[100:103], %[c0]\n\tv_mfma_f32_16x16x32_bf16 %[c1], v[148:151], v[100:103], %[c1]\n\t" \
    "v_mfma_f32_16x16x32_bf16 %[c2], v[152:155], v[100:103], %[c2]\n\tv_mfma_f32_16x16x32_bf16 %[c3], v[156:159], v[100:103], %[c3]\n\ts_waitcnt vmcnt(5) lgkmcnt(15)\n\tv_mfma_f32_16x16x32_bf16 %[c0], v[160:163], v[104:107], %[c0]\n\t" \
    "v_mfma_f32_16x16x32_bf16 %[c1], v[164:167], v[104:107], %[c1]\n\tv_mfma_f32_16x16x32_bf16 %[c2], v[168:171], v[104:107], %[c2]\n\tv_mfma_f32_16x16x32_bf16 %[c3], v[172:175], v[104:107], %[c3]\n\ts_waitcnt vmcnt(4) lgkmcnt(15)\n\t" \
    "v_mfma_f32_16x16x32_bf16 %[c0], v[176:179], v[108:111], %[c0]\n\tv_mfma_f32_16x16x32_bf16 %[c1], v[180:183], v[108:111], %[c1]\n\tv_mfma_f32_16x16x32_bf16 %[c2], v[184:187], v[108:111], %[c2]\n\tv_mfma_f32_16x16x32_bf16 %[c3], v[188:191], v[108:111], %[c3]\n\t" \
    "s_waitcnt vmcnt(3) lgkmcnt(15)\n\tv_mfma_f32_16x16x32_bf16 %[c0], v[192:195], v[112:115], %[c0]\n\tv_mfma_f32_16x16x32_bf16 %[c1], v[196:199], v[112:115], %[c1]\n\tv_mfma_f32_16x16x32_bf16 %[c2], v[200:203], v[112:115], %[c2]\n\t" \
    "v_mfma_f32_16x16x32_bf16 %[c3], v[204:207], v[112:115], %[c3]\n\ts_waitcnt vmcnt(2) lgkmcnt(15)\n\tv_mfma_f32_16x16x32_bf16 %[c0], v[208:211], v[116:119], %[c0]\n\tv_mfma_f32_16x16x32_bf16 %[c1], v[212:215], v[116:119], %[c1]\n\t" \
    "v_mfma_f32_16x16x32_bf16 %[c2], v[216:219], v[116:119], %[c2]\n\tv_mfma_f32_16x16x32_bf16 %[c3], v[220:223], v[116:119], %[c3]\n\ts_waitcnt vmcnt(1) lgkmcnt(8)\n\tv_mfma_f32_16x16x32_bf16 %[c0], v[224:227], v[120:123], %[c0]\n\t" \
    "v_mfma_f32_16x16x32_bf16 %[c1], v[228:231], v[120:123], %[c1]\n\tv_mfma_f32_16x16x32_bf16 %[c2], v[232:235], v[120:123], %[c2]\n\tv_mfma_f32_16x16x32_bf16 %[c3], v[236:239], v[120:123], %[c3]\n\ts_waitcnt vmcnt(0) lgkmcnt(0)\n\t" \
    "v_mfma_f32_16x16x32_bf16 %[c0], v[240:243], v[124:127], %[c0]\n\tv_mfma_f32_16x16x32_bf16 %[c1], v[244:247], v[124:127], %[c1]\n\tv_mfma_f32_16x16x32_bf16 %[c2], v[248:251], v[124:127], %[c2]\n\tv_mfma_f32_16x16x32_bf16 %[c3], v[252:255], v[124:127], %[c3]\n\t" \
    "s_nop 7\n\t"
#define CLOB_P "v64","v65","v66","v67","v68","v69","v70","v71","v72","v73","v74","v75","v76","v77","v78","v79","v80","v81","v82","v83","v84","v85","v86","v87","v88","v89","v90","v91","v92","v93","v94","v95","v96","v97","v98","v99","v100","v101","v102","v103","v104","v105","v106","v107","v108","v109","v110","v111","v112","v113","v114","v115","v116","v117","v118","v119","v120","v121","v122","v123","v124","v125","v126","v127","v128","v129","v130","v131","v132","v133","v134","v135","v136","v137","v138","v139","v140","v141","v142","v143","v144","v145","v146","v147","v148","v149","v150","v151"
#define CLOB_Q "v64","v65","v66","v67","v68","v69","v70","v71","v72","v73","v74","v75","v76","v77","v78","v79","v80","v81","v82","v83","v84","v85","v86","v87","v88","v89","v90","v91","v92","v93","v94","v95","v96","v97","v98","v99","v100","v101","v102","v103","v104","v105","v106","v107","v108","v109","v110","v111","v112","v113","v114","v115","v116","v117","v118","v119","v120","v121","v122","v123","v124","v125","v126","v127","v128","v129","v130","v131","v132","v133","v134","v135"
#define CLOB_RING "v128","v129","v130","v131","v132","v133","v134","v135","v136","v137","v138","v139","v140","v141","v142","v143","v144","v145","v146","v147","v148","v149","v150","v151","v152","v153","v154","v155","v156","v157","v158","v159"

__global__ __launch_bounds__(512, 2) void fused_kernel(const char* xA, const char* wA, const char* woA, const char* xB, const char* wB, const char* woB,
                                                        char* out, char* ws) {
    extern __shared__ __attribute__((aligned(16))) char smem[];
    lds_ptr lds = (lds_ptr)smem;
    const unsigned lds0 = (unsigned)(uintptr_t)lds;
    const int tid = threadIdx.x, lane = tid & 63, wave = __builtin_amdgcn_readfirstlane(tid >> 6);
    const int r = lane & 31, hh = lane >> 5, g = lane >> 4, tk = lane & 15;
    const int bx = blockIdx.x & 7, bi = blockIdx.x >> 3;
    asm volatile(".Lcw_from:");
    const bool isA = bi & 1;
    const int cg = (bx & 1) * 4 + ((bi >> 1) & 3), t = (bx >> 1) * 4 + (bi >> 3), row0 = t * 128;
    u64 tag;
    const int w3 = wave & 3, hp = w3 >> 1;
    u32x4 qa0, qa1;
    unsigned oao = 0;
    auto window_attention = [&]() {
        WAIT_LGKM0_BAR();
        const int j = w3 & 1, p = wave >> 2;
        if (wave >= 4) { qa0 = *(const LAS u32x4*)(lds + IMG_Q + w3 * 2048 + lane * 16); qa1 = *(const LAS u32x4*)(lds + IMG_Q + w3 * 2048 + 1024 + lane * 16); }
        lds_ptr kp = lds + IMG_K + ((2 * hp + hh) * 4 + p) * 1152 + (32 * j + r) * 16;
        lds_ptr vp = lds + IMG_V + ((2 * hp + hh) * 16 + (lane & 3)) * 288 + p * 144 + (32 * j + (r & 28)) * 2;
        u32x4 kf[3][2]; u32x2 vf[4][3];
#pragma unroll
        for (int T = 0; T < 3; ++T) { kf[T][0] = *(const LAS u32x4*)(kp + T * 64); kf[T][1] = *(const LAS u32x4*)(kp + T * 64 + 2304); }
#pragma unroll
        for (int D = 0; D < 4; ++D)
#pragma unroll
            for (int T = 0; T < 3; ++T) vf[D][T] = *(const LAS u32x2*)(vp + D * 1152 + T * 8);
        f32x4 S[3];
#define H4(v, c) __builtin_bit_cast(s16x4, u32x2{v[c], v[c + 1]})
#pragma unroll
        for (int T = 0; T < 3; ++T) {
            f32x4 s = MFMA4(H4(kf[T][0], 0), H4(qa0, 0), (f32x4{0.f, 0.f, 0.f, 0.f}));
            s = MFMA4(H4(kf[T][0], 2), H4(qa0, 2), s); s = MFMA4(H4(kf[T][1], 0), H4(qa1, 0), s); S[T] = MFMA4(H4(kf[T][1], 2), H4(qa1, 2), s);
        }
        const int jq = lane & 3;
        float lg[8];
#pragma unroll
        for (int i = 0; i < 4; ++i) { lg[i] = (i <= jq) ? S[2][i] : S[0][i]; lg[4 + i] = S[1][i]; }
#pragma unroll
        for (int i = 0; i < 8; i += 2) { const unsigned pk = cvtpk(lg[i], lg[i + 1]); lg[i] = __uint_as_float(pk << 16); lg[i + 1] = __uint_as_float(pk & 0xffff0000u); }
        if (t == 0 && j == 0) {
#pragma unroll
            for (int i = 0; i < 4; ++i) { if (i > jq && r < 8) lg[i] = -4.0e9f; if (r < 4) lg[4 + i] = -4.0e9f; }
        }
        const float mx = fmaxf(fmaxf(fmaxf(lg[0], lg[1]), fmaxf(lg[2], lg[3])), fmaxf(fmaxf(lg[4], lg[5]), fmaxf(lg[6], lg[7])));
        const float nm = -mx * QSCALE;
        float sum = 0.f;
#pragma unroll
        for (int i = 0; i < 8; ++i) { lg[i] = __builtin_amdgcn_exp2f(fmaf(lg[i], QSCALE, nm)); sum += lg[i]; }
        const float inv = __builtin_amdgcn_rcpf(sum);
        const u32x2 pm = {cvtpk(lg[0] * inv, lg[1] * inv), cvtpk(lg[2] * inv, lg[3] * inv)}, p1 = {cvtpk(lg[4] * inv, lg[5] * inv), cvtpk(lg[6] * inv, lg[7] * inv)};
        const u32x2 mk = {jq >= 1 ? 0xffffffffu : 0x0000ffffu, jq >= 3 ? 0xffffffffu : (jq == 2 ? 0x0000ffffu : 0u)};
        const u32x2 p2 = pm & mk, p0 = pm & ~mk;
        f32x4 O[4];
#pragma unroll
        for (int D = 0; D < 4; ++D) {
            f32x4 o = MFMA4(__builtin_bit_cast(s16x4, vf[D][0]), __builtin_bit_cast(s16x4, p0), (f32x4{0.f, 0.f, 0.f, 0.f}));
            o = MFMA4(__builtin_bit_cast(s16x4, vf[D][1]), __builtin_bit_cast(s16x4, p1), o);
            O[D] = MFMA4(__builtin_bit_cast(s16x4, vf[D][2]), __builtin_bit_cast(s16x4, p2), o);
        }
        const unsigned oo = WS_OA + ((t * 4 + 2 * j + p) * 32 + cg * 4 + hp * 2 + hh) * 1024 + r * 16;
        qa0 = u32x4{cvtpk(O[0][0], O[0][1]), cvtpk(O[0][2], O[0][3]), cvtpk(O[2][0], O[2][1]), cvtpk(O[2][2], O[2][3])};
        qa1 = u32x4{cvtpk(O[1][0], O[1][1]), cvtpk(O[1][2], O[1][3]), cvtpk(O[3][0], O[3][1]), cvtpk(O[3][2], O[3][3])};
        oao = oo;
        WAIT_LGKM0_BAR();
    };
    const int h = bx * 4 + (bi & 3), qb = bi >> 2;
    const int rt = bx * 4 + (bi >> 3), cs = bi & 7;
#define KVG (ws + WS_KV + (size_t)h * 131072)
#define FKP (ws + WS_FK + (h >> 1) * 32 * FS)
    if (wave < 4) {
        {
        if (wave == 0) { *(volatile LAS u32x2*)(lds + L_SIG) = u32x2{0u, 0u}; asm volatile("s_waitcnt lgkmcnt(0)" ::: "memory"); }
        unsigned cw = 0;
        if (wave == 1) {
            u64 pc; unsigned back, len;
            asm volatile("s_getpc_b64 %0\n.Lcw_here:\n\ts_mov_b32 %1, .Lcw_here-.Lcw_from\n\ts_mov_b32 %2, .Lcw_to-.Lcw_from" : "=s"(pc), "=s"(back), "=s"(len));
            const unsigned co = (unsigned)(bi * CWL + (lane % CWL)) * 128;
            asm volatile("global_load_dword %0, %1, %2" : "=v"(cw) : "v"(co < len ? co : 0u), "s"(pc - back) : "memory");
        }
        const int mp = wave & 1, row = 64 * mp + tk, q4 = (lane >> 2) & 3;
        unsigned x0 = lds0 + row * 128 + ((g ^ (tk >> 1)) << 4), x1 = lds0 + row * 128 + (((4 + g) ^ (tk >> 1)) << 4);
        unsigned w0 = lds0 + 2 * g * 1536 + q4 * 128 + ((hp ^ (q4 >> 1)) * 64) + (g & 1) * 32 + (lane & 3) * 8, w1 = w0 ^ 32;
        unsigned cnt = 8, ph = 0, dl;
        f32x16 aq0 = {}, aq1 = {}, ak0 = {}, ak1 = {}, av0 = {}, av1 = {};
#define P_OUT "+{v[160:175]}"(aq0), "+{v[176:191]}"(aq1), "+{v[192:207]}"(ak0), "+{v[208:223]}"(ak1), "+{v[224:239]}"(av0), "+{v[240:255]}"(av1), \
              [x0] "+v"(x0), [x1] "+v"(x1), [w0] "+v"(w0), [w1] "+v"(w1), [cnt] "+s"(cnt), [ph] "+s"(ph), [dl] "=&s"(dl), [ep] "=&s"(tag)
#define P_IN [pos] "s"(SLOT), [neg] "s"(-2 * SLOT), [ws] "s"(ws)
        auto frag = [](const f32x16& a, int T) -> u32x4 {
            auto s0 = __builtin_amdgcn_permlane16_swap(cvtpk(a[8 * T], a[8 * T + 1]), cvtpk(a[8 * T + 4], a[8 * T + 5]), false, false);
            auto s1 = __builtin_amdgcn_permlane16_swap(cvtpk(a[8 * T + 2], a[8 * T + 3]), cvtpk(a[8 * T + 6], a[8 * T + 7]), false, false);
            return u32x4{s0[0], s1[0], s0[1], s1[1]};
        };
        if (isA) {
            unsigned r0 = lds0 + SL_HALO + tk * 128 + ((g ^ (tk >> 1)) << 4), r1 = lds0 + SL_HALO + tk * 128 + (((4 + g) ^ (tk >> 1)) << 4);
            f32x4 hk = {0.f, 0.f, 0.f, 0.f}, hv = {0.f, 0.f, 0.f, 0.f};
            if (mp) asm volatile("s_load_dwordx2 %[ep], %[ws], 0x0\n\ts_nop 1\n\t" PROJ_LOOP_A1
                         : P_OUT, "+{v[152:155]}"(hk), "+{v[156:159]}"(hv), [r0] "+v"(r0), [r1] "+v"(r1) : P_IN : "memory", "scc", CLOB_P);
            else asm volatile("s_load_dwordx2 %[ep], %[ws], 0x0\n\ts_nop 1\n\t" PROJ_LOOP_A0
                         : P_OUT, "+{v[152:155]}"(hk), "+{v[156:159]}"(hv), [r0] "+v"(r0), [r1] "+v"(r1) : P_IN : "memory", "scc", CLOB_P);
            tag += 1;
            asm volatile("s_waitcnt vmcnt(0)" :: "v"(cw) : "memory");
            lds_ptr kw = lds + IMG_K + (4 * hp + hh) * 2304 + (8 + 32 * mp + r) * 16;
            *(LAS u32x4*)kw = frag(ak0, 0); *(LAS u32x4*)(kw + 4608) = frag(ak1, 0);
            *(LAS u32x4*)(kw + 1152) = frag(ak0, 1); *(LAS u32x4*)(kw + 1152 + 4608) = frag(ak1, 1);
            lds_ptr vw = lds + IMG_V + (2 * hp * 16 + tk) * 288 + (8 + 32 * mp + 4 * g) * 2;
#pragma unroll
            for (int i = 0; i < 4; ++i) {
                *(LAS u32x2*)(vw + (i >> 1) * 144 + (i & 1) * 32) = u32x2{cvtpk(av0[4 * i], av0[4 * i + 1]), cvtpk(av0[4 * i + 2], av0[4 * i + 3])};
                *(LAS u32x2*)(vw + 16 * 288 + (i >> 1) * 144 + (i & 1) * 32) = u32x2{cvtpk(av1[4 * i], av1[4 * i + 1]), cvtpk(av1[4 * i + 2], av1[4 * i + 3])};
            }
            const int hd = 2 * hp + mp;
            *(LAS u32x2*)(lds + IMG_K + ((hd * 2 + (g >> 1)) * 2 + (tk >> 3)) * 1152 + (tk & 7) * 16 + (g & 1) * 8) = u32x2{cvtpk(hk[0], hk[1]), cvtpk(hk[2], hk[3])};
            *(LAS u32x2*)(lds + IMG_V + (hd * 16 + tk) * 288 + (g >> 1) * 144 + (g & 1) * 8) = u32x2{cvtpk(hv[0], hv[1]), cvtpk(hv[2], hv[3])};
            qa0 = frag(aq0, 0); qa1 = frag(aq1, 0);
            u32x4 qb0 = frag(aq0, 1), qb1 = frag(aq1, 1);
#pragma unroll
            for (int c = 0; c < 4; ++c) {
                auto sw = __builtin_amdgcn_permlane32_swap(qa0[c], qa1[c], false, false); qa0[c] = sw[0]; qa1[c] = sw[1];
                auto sv = __builtin_amdgcn_permlane32_swap(qb0[c], qb1[c], false, false); qb0[c] = sv[0]; qb1[c] = sv[1];
            }
            *(LAS u32x4*)(lds + IMG_Q + wave * 2048 + lane * 16) = qb0; *(LAS u32x4*)(lds + IMG_Q + wave * 2048 + 1024 + lane * 16) = qb1;
        } else {
            const unsigned xs0 = x0, xs1 = x1, ws0 = w0 + SL_W, ws1 = w1 + SL_W;
            cnt = 5;
            asm volatile("s_load_dwordx2 %[ep], %[ws], 0x0\n\ts_nop 1\n\t" PROJ_B_KV : P_OUT : P_IN : "memory", "scc", CLOB_P);
            tag += 1;
            asm volatile("s_waitcnt vmcnt(0)" :: "v"(cw) : "memory");
            const int tile = t * 4 + 2 * mp, h0 = cg * 4 + hp * 2;
            const unsigned ko = WS_KV + (h0 * 64 + tile) * 2048 + lane * 16;
            const u32x4 k00 = frag(ak0, 0), k01 = frag(ak0, 1), k10 = frag(ak1, 0), k11 = frag(ak1, 1);
            gst16_sc1(ws, ko, k00); gst16_sc1(ws, ko + 2048, k01); gst16_sc1(ws, ko + 131072, k10); gst16_sc1(ws, ko + 2048 + 131072, k11);
            gst16_sc1(ws, ko + 1024, frag(av0, 0)); gst16_sc1(ws, ko + 3072, frag(av0, 1));
            gst16_sc1(ws, ko + 1024 + 131072, frag(av1, 0)); gst16_sc1(ws, ko + 3072 + 131072, frag(av1, 1));
            float n0 = 0.f, n1 = 0.f, n2 = 0.f, n3 = 0.f;
#pragma unroll
            for (int c = 0; c < 4; ++c) { n0 = dot2(k00[c], k00[c], n0); n1 = dot2(k10[c], k10[c], n1); n2 = dot2(k01[c], k01[c], n2); n3 = dot2(k11[c], k11[c], n3); }
            auto sa = __builtin_amdgcn_permlane32_swap(__float_as_uint(n0), __float_as_uint(n1), false, false);
            auto sb = __builtin_amdgcn_permlane32_swap(__float_as_uint(n2), __float_as_uint(n3), false, false);
            const float n = max32(fmaxf(__uint_as_float(sa[0]) + __uint_as_float(sa[1]), __uint_as_float(sb[0]) + __uint_as_float(sb[1])));
            if (r == 0) gst4_sc1(ws, WS_KN + ((h0 + hh) * 32 + t * 2 + mp) * 4, __float_as_uint(n));
            asm volatile(PROJ_B_Q : "+{v[160:175]}"(aq0), "+{v[176:191]}"(aq1)
                         : [x0] "v"(xs0), [x1] "v"(xs1), [w0] "v"(ws0), [w1] "v"(ws1),
                           [x0h] "v"(xs0 + 2 * SLOT), [x1h] "v"(xs1 + 2 * SLOT), [w0h] "v"(ws0 + 2 * SLOT), [w1h] "v"(ws1 + 2 * SLOT) : "memory", CLOB_Q);
            const unsigned fi = (((cg * 2 + hp) * 16 + t) * 2 + mp) * FS;
            const unsigned qo = WS_Q + (h0 * 64 + tile) * 1024 + lane * 16;
            gst16_sc1(ws, qo, frag(aq0, 0)); gst16_sc1(ws, qo + 1024, frag(aq0, 1));
            gst16_sc1(ws, qo + 65536, frag(aq1, 0)); gst16_sc1(ws, qo + 1024 + 65536, frag(aq1, 1));
            asm volatile("s_waitcnt vmcnt(4)" ::: "memory");
            if (lane == 0) put_flag(ws + WS_FK + fi, tag);
            asm volatile("s_waitcnt vmcnt(1)" ::: "memory");
            if (lane == 0) put_flag(ws + WS_FQ + fi, tag);
        }
        }
        if (isA) window_attention();
        const char* kvg = KVG; const char* fk = FKP;
        __builtin_amdgcn_s_setprio(3);
        const int qh = wave & 1, half = wave >> 1, qt0 = qb * 8 + 4 * qh;
        const unsigned padv = (r == 16) ? 0x3F803F80u : 0u;
        const u32x4 pad = {padv, padv, padv, padv};
        unsigned ad = lds0 + half * 2048 + lane * 16, idx = 0xDDDDu;
        u32x4 ka, kb, v0a = pad, v1a = pad, v0b = pad, v1b = pad;
        f32x16 o0 = {}, o1 = {}, o2 = {}, o3 = {}, sy = {}, sx;
        unsigned qoff = WS_Q + (h * 64 + qt0 + 2 * half) * 1024 + lane * 16, qof2 = qoff ^ 2048, knoff = WS_KN + (h * 32 + r) * 4;
        asm volatile("" : "+{v[96:111]}"(o0), "+{v[112:127]}"(o1), "+{v[128:143]}"(o2), "+{v[144:159]}"(o3), "+{v[80:95]}"(sy), "+{v[32:35]}"(v0a), "+{v[40:43]}"(v1a), "+{v[36:39]}"(v0b), "+{v[44:47]}"(v1b), "+v"(ad), "+v"(idx), "+v"(qoff), "+v"(qof2), "+v"(knoff));
        unsigned cwa = lds0 + L_CMB + wave * 4608 + lane * 16, cra = lds0 + L_CMB + (wave ^ 2) * 4608 + lane * 16;
        unsigned lwa = cwa + 4096 - lane * 8, lra = cra + 4096 - lane * 8;
        unsigned oo = WS_OB + ((qt0 + 2 * half) * 32 + h) * 1024 + lane * 16, fo_ = WS_FO + ((qt0 + 2 * half + (lane & 1)) * 32 + h) * FS, go = lds0 + L_GO + wave * 4, one = 1;
        u64 tv = tag, bad; u32x4 r0, r1;
        asm volatile("" : "+v"(cwa), "+v"(cra), "+v"(lwa), "+v"(lra), "+v"(oo), "+v"(fo_), "+v"(go), "+v"(one), "+v"(tv));
        if (wave == 0) { poll_flags(fk + (WS_FQ - WS_FK) + 4 * qb * FS, (lane & 3) * FS, tag); *(volatile LAS unsigned*)(lds + L_SIG + 4) = 1u; }
        lds_wait2(lds + L_SIG);
        u32x4 qv0, qv1, qv2, qv3; unsigned knb;
        asm volatile("global_load_dword %4, %6, %7 sc1\n\tglobal_load_dwordx4 %0, %5, %7 sc1\n\tglobal_load_dwordx4 %1, %5, %7 offset:1024 sc1\n\t"
                     "global_load_dwordx4 %2, %8, %7 sc1\n\tglobal_load_dwordx4 %3, %8, %7 offset:1024 sc1\n\ts_waitcnt vmcnt(4)"
                     : "=&{v[8:11]}"(qv0), "=&{v[12:15]}"(qv1), "=&{v[16:19]}"(qv2), "=&{v[20:23]}"(qv3), "=&v"(knb) : "v"(qoff), "v"(knoff), "s"(ws), "v"(qof2) : "memory");
        float kn = max32(__uint_as_float(knb));
        asm volatile("s_waitcnt vmcnt(0)" : "+{v[8:11]}"(qv0), "+{v[12:15]}"(qv1), "+{v[16:19]}"(qv2), "+{v[20:23]}"(qv3), "+v"(kn) :: "memory");
        auto rescale = [](u32x4& v) {
#pragma unroll
            for (int c = 0; c < 4; ++c) {
                const f32x2 t2 = f32x2{__uint_as_float(v[c] << 16), __uint_as_float(v[c] & 0xffff0000u)} * QSCALE;
                v[c] = cvtpk(t2[0], t2[1]);
            }
        };
        float n0 = dot2(qv0[0], qv0[0], 0.f), n1 = dot2(qv1[0], qv1[0], 0.f), n2 = dot2(qv2[0], qv2[0], 0.f), n3 = dot2(qv3[0], qv3[0], 0.f);
#pragma unroll
        for (int c = 1; c < 4; ++c) { n0 = dot2(qv0[c], qv0[c], n0); n1 = dot2(qv1[c], qv1[c], n1); n2 = dot2(qv2[c], qv2[c], n2); n3 = dot2(qv3[c], qv3[c], n3); }
        const float qn = fmaxf(fmaxf(n0, n1), fmaxf(n2, n3)) * (kn * (QSCALE * QSCALE));
        rescale(qv0);
        const bf16x8 q0 = __builtin_bit_cast(bf16x8, qv0);
        bf16x8 q1 = __builtin_bit_cast(bf16x8, qv1), q2 = __builtin_bit_cast(bf16x8, qv2), q3 = __builtin_bit_cast(bf16x8, qv3);
        const f32x2 qs2 = {QSCALE, QSCALE};
        f32x16 ci;
        asm volatile("s_barrier\n\t"
                     "ds_read_b128 %[ka], %[ad]\n\tds_read_b128 %[kb], %[ad] offset:4096\n\t"
                     "s_mov_b64 exec, %[msk]\n\tds_read_b128 %[v0a], %[ad] offset:1024\n\tds_read_b128 %[v1a], %[ad] offset:1280\n\ts_mov_b64 exec, -1\n\t"
                     "v_mov_b32 v48, %[qn]\n\tv_mov_b32 v49, %[qn]\n\ts_nop 1\n\tv_permlane32_swap_b32 v48, v49\n\tv_add_f32 v48, v48, v49\n\tv_sqrt_f32 v48, v48\n\t"
                     "v_mov_b32 v49, 0x42700000\n\tv_fmac_f32 v49, 0xbf820000, v48\n\tv_mov_b32 v48, v49\n\t"
                     "v_mov_b64 v[50:51], v[48:49]\n\tv_mov_b64 v[52:53], v[48:49]\n\tv_mov_b64 v[54:55], v[48:49]\n\tv_mov_b64 v[56:57], v[48:49]\n\tv_mov_b64 v[58:59], v[48:49]\n\tv_mov_b64 v[60:61], v[48:49]\n\tv_mov_b64 v[62:63], v[48:49]\n\t"
                     "v_lshlrev_b32 v168, 16, v12\n\tv_and_b32 v169, 0xffff0000, v12\n\tv_lshlrev_b32 v170, 16, v13\n\tv_and_b32 v171, 0xffff0000, v13\n\tv_lshlrev_b32 v172, 16, v14\n\tv_and_b32 v173, 0xffff0000, v14\n\tv_lshlrev_b32 v174, 16, v15\n\tv_and_b32 v175, 0xffff0000, v15\n\tv_pk_mul_f32 v[168:169], v[168:169], %[qs] op_sel_hi:[1,0]\n\tv_pk_mul_f32 v[170:171], v[170:171], %[qs] op_sel_hi:[1,0]\n\tv_pk_mul_f32 v[172:173], v[172:173], %[qs] op_sel_hi:[1,0]\n\tv_pk_mul_f32 v[174:175], v[174:175], %[qs] op_sel_hi:[1,0]\n\tv_cvt_pk_bf16_f32 v12, v168, v169\n\tv_cvt_pk_bf16_f32 v13, v170, v171\n\tv_cvt_pk_bf16_f32 v14, v172, v173\n\tv_cvt_pk_bf16_f32 v15, v174, v175\n\t"
                     "s_waitcnt lgkmcnt(3)\n\tv_mfma_f32_32x32x16_bf16 v[64:79], %[ka], %[q0], %[ci]\n\t"
                     "s_cmp_eq_u32 %[isa], 0\n\ts_cbranch_scc1 7f\n\t"
                     "global_store_dwordx4 %[oao], %[qa0], %[ws] sc1\n\tglobal_store_dwordx4 %[oao], %[qa1], %[ws] offset:512 sc1\n\t"
                     "7:\n\t"
                     "v_lshlrev_b32 v168, 16, v16\n\tv_and_b32 v169, 0xffff0000, v16\n\tv_lshlrev_b32 v170, 16, v17\n\tv_and_b32 v171, 0xffff0000, v17\n\tv_lshlrev_b32 v172, 16, v18\n\tv_and_b32 v173, 0xffff0000, v18\n\tv_lshlrev_b32 v174, 16, v19\n\tv_and_b32 v175, 0xffff0000, v19\n\tv_pk_mul_f32 v[168:169], v[168:169], %[qs] op_sel_hi:[1,0]\n\tv_pk_mul_f32 v[170:171], v[170:171], %[qs] op_sel_hi:[1,0]\n\tv_pk_mul_f32 v[172:173], v[172:173], %[qs] op_sel_hi:[1,0]\n\tv_pk_mul_f32 v[174:175], v[174:175], %[qs] op_sel_hi:[1,0]\n\tv_cvt_pk_bf16_f32 v16, v168, v169\n\tv_cvt_pk_bf16_f32 v17, v170, v171\n\tv_cvt_pk_bf16_f32 v18, v172, v173\n\tv_cvt_pk_bf16_f32 v19, v174, v175\n\t"
                     "v_lshlrev_b32 v168, 16, v20\n\tv_and_b32 v169, 0xffff0000, v20\n\tv_lshlrev_b32 v170, 16, v21\n\tv_and_b32 v171, 0xffff0000, v21\n\tv_lshlrev_b32 v172, 16, v22\n\tv_and_b32 v173, 0xffff0000, v22\n\tv_lshlrev_b32 v174, 16, v23\n\tv_and_b32 v175, 0xffff0000, v23\n\tv_pk_mul_f32 v[168:169], v[168:169], %[qs] op_sel_hi:[1,0]\n\tv_pk_mul_f32 v[170:171], v[170:171], %[qs] op_sel_hi:[1,0]\n\tv_pk_mul_f32 v[172:173], v[172:173], %[qs] op_sel_hi:[1,0]\n\tv_pk_mul_f32 v[174:175], v[174:175], %[qs] op_sel_hi:[1,0]\n\tv_cvt_pk_bf16_f32 v20, v168, v169\n\tv_cvt_pk_bf16_f32 v21, v170, v171\n\tv_cvt_pk_bf16_f32 v22, v172, v173\n\tv_cvt_pk_bf16_f32 v23, v174, v175\n\t"
                     "s_waitcnt lgkmcnt(0)\n\t"
                     ATTN_BODY
                     "s_nop 1\n\t"
                     "v_smfmac_f32_32x32x32_bf16 %[o3], %[v1b], v[88:95], %[idx]\n\t"
                     "ds_write_b128 %[cwa], v[128:131]\n\tds_write_b128 %[cwa], v[132:135] offset:1024\n\ts_nop 9\n\t"
                     "ds_write_b128 %[cwa], v[144:147] offset:2048\n\tds_write_b128 %[cwa], v[148:151] offset:3072\n\tds_write2_b32 %[lwa], v136, v152 offset1:1\n\t"
                     "s_waitcnt lgkmcnt(0)\n\ts_barrier\n\t"
                     "ds_read_b64 v[66:67], %[lra]\n\tds_read_b128 v[68:71], %[cra]\n\tds_read_b128 v[72:75], %[cra] offset:1024\n\t"
                     "ds_read_b128 v[76:79], %[cra] offset:2048\n\tds_read_b128 v[80:83], %[cra] offset:3072\n\t"
                     "s_waitcnt lgkmcnt(4)\n\tv_add_f32 v66, v66, v104\n\tv_add_f32 v67, v67, v120\n\tv_mov_b32 v64, v66\n\tv_mov_b32 v65, v67\n\t"
                     "s_waitcnt lgkmcnt(3)\n\t"
                     "v_pk_add_f32 v[96:97], v[96:97], v[68:69]\n\t""v_pk_add_f32 v[98:99], v[98:99], v[70:71]\n\t"
                     "v_permlane32_swap_b32 v66, v64\n\tv_permlane32_swap_b32 v67, v65\n\ts_waitcnt lgkmcnt(2)\n\t"
                     "v_pk_add_f32 v[100:101], v[100:101], v[72:73]\n\t""v_pk_add_f32 v[66:67], v[66:67], v[64:65]\n\t""v_pk_add_f32 v[102:103], v[102:103], v[74:75]\n\t"
                     "v_rcp_f32 v64, v66\n\tv_rcp_f32 v84, v67\n\tv_min_f32 v86, v66, v67\n\tv_max_f32 v87, v66, v67\n\ts_waitcnt lgkmcnt(0)\n\t"
                     "v_pk_add_f32 v[112:113], v[112:113], v[76:77]\n\t""v_pk_add_f32 v[114:115], v[114:115], v[78:79]\n\t""v_pk_add_f32 v[116:117], v[116:117], v[80:81]\n\t""v_pk_add_f32 v[118:119], v[118:119], v[82:83]\n\t"
                     "v_cmp_nle_f32 vcc, %[lo], v86\n\tv_cmp_nge_f32 %[bad], %[hi], v87\n\t"
                     "v_pk_mul_f32 v[96:97], v[96:97], v[64:65] op_sel_hi:[1,0]\n\t""v_pk_mul_f32 v[112:113], v[112:113], v[84:85] op_sel_hi:[1,0]\n\t"
                     "v_pk_mul_f32 v[98:99], v[98:99], v[64:65] op_sel_hi:[1,0]\n\t""v_pk_mul_f32 v[114:115], v[114:115], v[84:85] op_sel_hi:[1,0]\n\t"
                     "v_pk_mul_f32 v[100:101], v[100:101], v[64:65] op_sel_hi:[1,0]\n\t""v_pk_mul_f32 v[116:117], v[116:117], v[84:85] op_sel_hi:[1,0]\n\t"
                     "v_pk_mul_f32 v[102:103], v[102:103], v[64:65] op_sel_hi:[1,0]\n\t""v_pk_mul_f32 v[118:119], v[118:119], v[84:85] op_sel_hi:[1,0]\n\t"
                     "s_or_b64 %[bad], %[bad], vcc\n\tv_cmp_u_f32 vcc, v66, v67\n\t"
                     "v_cvt_pk_bf16_f32 v164, v96, v97\n\tv_cvt_pk_bf16_f32 v165, v98, v99\n\tv_cvt_pk_bf16_f32 v166, v100, v101\n\tv_cvt_pk_bf16_f32 v167, v102, v103\n\t"
                     "v_cvt_pk_bf16_f32 v160, v112, v113\n\tv_cvt_pk_bf16_f32 v161, v114, v115\n\tv_cvt_pk_bf16_f32 v162, v116, v117\n\tv_cvt_pk_bf16_f32 v163, v118, v119\n\t"
                     "s_or_b64 %[bad], %[bad], vcc"
                     : [o0] "+{v[96:111]}"(o0), [o1] "+{v[112:127]}"(o1), [o2] "+{v[128:143]}"(o2), [o3] "+{v[144:159]}"(o3), "=&{v[64:79]}"(sx), "+{v[80:95]}"(sy), [ka] "=&{v[24:27]}"(ka), [kb] "=&{v[28:31]}"(kb),
                       [v0a] "+{v[32:35]}"(v0a), [v0b] "+{v[36:39]}"(v0b), [v1a] "+{v[40:43]}"(v1a), [v1b] "+{v[44:47]}"(v1b), [ad] "+v"(ad), "=&{v[164:167]}"(r0), "=&{v[160:163]}"(r1), [bad] "=&s"(bad), [ci] "=&{v[48:63]}"(ci),
                       [q1] "+{v[12:15]}"(q1), [q2] "+{v[16:19]}"(q2), [q3] "+{v[20:23]}"(q3)
                     : [cwa] "v"(cwa), [cra] "v"(cra), [lwa] "v"(lwa), [lra] "v"(lra), [lo] "s"(0x1p-96f), [hi] "s"(0x1p80f), [q0] "{v[8:11]}"(q0), [qs] "s"(qs2), [qn] "v"(qn), [isa] "s"((unsigned)isA), [oao] "v"(oao), [qa0] "v"(qa0), [qa1] "v"(qa1), [ws] "s"(ws), [idx] "v"(idx), [msk] "s"(0x0000FFFF0000FFFFull)
                     : "memory", "scc", "vcc", "v168", "v169", "v170", "v171", "v172", "v173", "v174", "v175");
        int lane_ = threadIdx.x; asm volatile("" : "+v"(lane_)); lane_ &= 63;
        const int r_ = lane_ & 31, hh_ = lane_ >> 5;
        if (__builtin_expect(bad != 0, 0)) {
#pragma unroll 1
            for (int u = 0; u < 2; ++u) {
            const unsigned pv_ = (r_ == 16) ? 0x3F803F80u : 0u; const u32x4 pad_ = {pv_, pv_, pv_, pv_};
            const bf16x8 q = u ? q1 : q0;
            f32x16 o; float l;
            float mx = -3.0e38f;
#pragma unroll 1
            for (int kt = 0; kt < 64; ++kt) {
                u32x4 k = gld16_sc1(kvg, kt * 2048 + lane_ * 16);
                asm volatile("s_waitcnt vmcnt(0)" : "+v"(k) :: "memory");
                const f32x16 s = MFMA32(__builtin_bit_cast(bf16x8, k), q, f32x16{});
#pragma unroll
                for (int i = 0; i < 16; ++i) mx = fmaxf(mx, s[i]);
            }
            mx = swap32_max(mx);
            f32x16 cm;
#pragma unroll
            for (int i = 0; i < 16; ++i) { cm[i] = -mx; o[i] = 0.f; }
#pragma unroll 1
            for (int kt = 0; kt < 64; ++kt) {
                u32x4 k = gld16_sc1(kvg, kt * 2048 + lane_ * 16);
                u32x4 va = gld16_sc1(kvg, kt * 2048 + 1024 + hh_ * 512 + (r_ & 15) * 16), vb = gld16_sc1(kvg, kt * 2048 + 1280 + hh_ * 512 + (r_ & 15) * 16);
                asm volatile("s_waitcnt vmcnt(0)" : "+v"(k), "+v"(va), "+v"(vb) :: "memory");
                if (r_ >= 16) { va = pad_; vb = pad_; }
                const f32x16 s = MFMA32(__builtin_bit_cast(bf16x8, k), q, cm);
                f32x8 pl, pu;
#pragma unroll
                for (int i = 0; i < 8; ++i) { pl[i] = __builtin_amdgcn_exp2f(s[i]); pu[i] = __builtin_amdgcn_exp2f(s[8 + i]); }
                o = SMFMAC(__builtin_bit_cast(bf16x8, va), __builtin_bit_cast(bf16x16, pl), o);
                o = SMFMAC(__builtin_bit_cast(bf16x8, vb), __builtin_bit_cast(bf16x16, pu), o);
            }
            l = swap32_add(o[8]);
            if (u) r1 = pack8(o, 0, __builtin_amdgcn_rcpf(l)); else r0 = pack8(o, 0, __builtin_amdgcn_rcpf(l));
            }
        }
        asm volatile("global_store_dwordx4 %0, v[164:167], %5 sc1\n\tv_add_u32 %0, 0x8000, %0\n\tglobal_store_dwordx4 %0, v[160:163], %5 sc1\n\tds_write_b32 %3, %4\n\t"
                     "s_waitcnt vmcnt(0)\n\ts_mov_b64 exec, 3\n\tglobal_store_dwordx2 %1, %2, %5 sc1\n\ts_mov_b64 exec, -1"
                     : "+v"(oo) : "v"(fo_), "v"(tv), "v"(go), "v"(one), "s"(ws), "{v[164:167]}"(r0), "{v[160:163]}"(r1) : "memory");
        asm volatile(".Lcw_to:");
        __builtin_amdgcn_endpgm();
    } else {
        {
        const unsigned ia = bi & 1, sh = 10u + ia, rs = 1u << sh;
        const unsigned xr = (unsigned)(row0 + 32 * w3 - 31 * (ia & w3)) * 1024 + ((lane >> 3) << sh) + 4096;
        const unsigned xe = xr + (((lane & 7) ^ (lane >> 4)) << 4), xd = xr + (((lane & 7) ^ (4 + (lane >> 4))) << 4);
        unsigned x0 = xe, x1 = xd + 8 * rs - 1024, x2 = xe + 16 * rs - 2048, x3 = xd + 24 * rs - 3072;
        unsigned xo = (((bx & 6) + 3 * (bx & 1) + 3 * ia + 4 * (((bi >> 3) ^ (bi >> 1)) & 1)) & 7) << 7;
        const int q = (lane >> 3) & 3;
        const unsigned wl = (unsigned)(w3 * 16 + q) * 3072 + cg * 128 + (((lane & 7) ^ ((q >> 1) << 2)) << 4) + 4096;
        unsigned w0 = wl + hh * 1024, w1 = wl + (hh ? 12288 : 2048) - 1024, w2 = wl + 12288 + 1024 + hh * 1024 - 2048;
        unsigned w3_ = (w0 ^ 32) + 24576 - 3072, w4 = (w1 ^ 32) + 1024 + 24576, w5 = (w2 ^ 32) + 2048 + 24576 - 1024;
        const int eta = 4 * w3 + q;
        int hrow = row0 - 16 + 2 * (eta & 7) + (eta >> 3); hrow = hrow < 0 ? 0 : hrow;
        unsigned ho = (unsigned)(hrow * 1024 + (((lane & 7) ^ (eta >> 1)) << 4)) + 4096;
        unsigned dx = lds0 + w3 * 4096, dw = SL_W + w3 * 2048, dh = SL_HALO - w3 * 3584, pi = 0, cnt = 5, dl;
        asm volatile("" : "+v"(x0), "+v"(x1), "+v"(x2), "+v"(x3), "+v"(w0), "+v"(w1), "+v"(w2), "+v"(w3_), "+v"(w4), "+v"(w5), "+v"(ho),
                          "+s"(xo), "+s"(dx), "+s"(dw), "+s"(dh), "+s"(pi), "+s"(cnt));
        __builtin_amdgcn_sched_barrier(0);
        u64 xp, wp;
        asm volatile("s_cmp_lg_u32 %2, 0\n\ts_cselect_b64 %0, %3, %4\n\ts_cselect_b64 %1, %5, %6" : "=&s"(xp), "=&s"(wp) : "s"(ia), "s"(xA), "s"(xB), "s"(wA), "s"(wB) : "scc");
        xp -= 4096; wp -= 4096;
        const unsigned xb = (unsigned)xp, xh = (unsigned)(xp >> 32), wb = (unsigned)wp, wh = (unsigned)(wp >> 32);
#define LD_IN [x0] "v"(x0), [x1] "v"(x1), [x2] "v"(x2), [x3] "v"(x3), [w0] "v"(w0), [w1] "v"(w1), [w2] "v"(w2), [w3] "v"(w3_), [w4] "v"(w4), [w5] "v"(w5), \
              [ws] "s"(ws), [xb] "s"(xb), [xh] "s"(xh), [wb] "s"(wb), [wh] "s"(wh)
        if (isA) {
            asm volatile("s_load_dwordx2 %[ep], %[ws], 0x0\n\t" LOADER_A
                         : [xo] "+s"(xo), [dx] "+s"(dx), [pi] "+s"(pi), [cnt] "+s"(cnt), [dl] "=&s"(dl), [ep] "=&s"(tag)
                         : LD_IN, [ho] "v"(ho), [dw] "s"(dw), [dh] "s"(dh), [pos] "s"(SLOT), [neg] "s"(-2 * SLOT)
                         : "memory", "scc", "m0", "s40", "s41", "s42", "s43");
        } else {
            asm volatile("s_load_dwordx2 %[ep], %[ws], 0x0\n\t" LOADER_B
                         : [xo] "+s"(xo), [dx] "+s"(dx), [pi] "+s"(pi), [cnt] "+s"(cnt), [dl] "=&s"(dl), [ep] "=&s"(tag)
                         : LD_IN, [dw] "s"(dw), [pos] "s"(SLOT), [neg] "s"(-2 * SLOT)
                         : "memory", "scc", "m0", "s40", "s41", "s42", "s43");
        }
        tag += 1;
        }
        if (isA) window_attention();
        const char* kvg = KVG; const char* fk = FKP;
        if (wave == 4) {
            if (!isA) __builtin_amdgcn_s_sleep(BSLEEP);
            poll_flags(fk, r * FS, tag);
            *(volatile LAS unsigned*)(lds + L_SIG) = 1u;
        } else lds_wait(lds + L_SIG);
        const unsigned l16 = lane * 16;
        auto kv = [&](int i) {
            const int k5 = 4 * i + w3;
            glds16x2_sc1(kvg + k5 * 2048, l16, lds0 + k5 * 4096); glds16x2_sc1(kvg + k5 * 2048 + 65536, l16, lds0 + k5 * 4096 + 2048);
        };
        kv(0); kv(1);
        wait_vm_bar<4>();
        kv(2); kv(3);
        wait_vm_bar<8>();
        if (isA) { gst16_sc1(ws, oao, qa0); gst16_sc1(ws, oao + 512, qa1); }
#pragma unroll 1
        for (int i = 4; i < 8; ++i) kv(i);
        wait_vm_bar<16>();
        if (isA && tid == 256) put_flag(ws + WS_FA + (t * 8 + cg) * FS, tag);
        wait_vm_bar<0>();
        const unsigned off = (lane >> 3) * 1024 + (((((lane >> 1) & 3) ^ ((lane >> 4) & 3)) << 5) | ((lane & 1) << 4));
        const unsigned ofo = ((2 * rt + (w3 >> 1)) * 32 + (g >> 1)) * 1024 + (g & 1) * 512 + (16 * (w3 & 1) + tk) * 16;
        typedef __attribute__((ext_vector_type(32))) float f32x32;
        f32x32 rw0, rw1;
#define LOAD_ROWS(P) asm volatile(ROWS_LD : "=&{v[64:95]}"(rw0), "=&{v[96:127]}"(rw1) \
                 : [of0] "v"(ofo), [of1] "v"(ofo + 4096), [of2] "v"(ofo + 8192), [of3] "v"(ofo + 12288), [o0] "s"(P), [o1] "s"((P) + 16384) : "memory")
        if (blockIdx.x == 0 && wave == 5) {
#pragma unroll 1
            for (unsigned spins = 0; spins < (1u << 20); ++spins) {
                u64 f[10];
#pragma unroll
                for (int i = 0; i < 10; ++i) f[i] = gld8_sc1(ws + WS_FQ + i * 64 * FS, lane * FS);
                asm volatile("s_waitcnt vmcnt(0)" : "+v"(f[0]), "+v"(f[1]), "+v"(f[2]), "+v"(f[3]), "+v"(f[4]), "+v"(f[5]), "+v"(f[6]), "+v"(f[7]), "+v"(f[8]), "+v"(f[9]) :: "memory");
                bool ok = true;
#pragma unroll
                for (int i = 0; i < 10; ++i) ok = ok && f[i] == tag;
                if (__all(ok)) break;
                __builtin_amdgcn_s_sleep(16);
            }
            if (lane == 0) put_flag(ws + WS_EPOCH, tag);
        }
        poll_flags<8>(ws + WS_FA + (rt >> 1) * 8 * FS, (lane & 7) * FS, tag);
        LOAD_ROWS(ws + WS_OA);
        WAIT_LGKM0_BAR();
#pragma unroll 1
        for (int i = 0; i < 16; ++i) {
            const int pc = w3 * 16 + i;
            glds16(woA + pc * 8192 + cs * 128, off, lds0 + L_WOA + pc * 1024);
            glds16(woB + pc * 8192 + cs * 128, off, lds0 + (w3 < 2 ? L_WOB0 : L_WOB1 - 32768) + pc * 1024);
        }
        WAIT_LGKM0_BAR();
        *(volatile LAS unsigned*)(lds + L_GO + w3 * 4) = 0u;
        const int q4 = (lane >> 2) & 3, fw = (2 * (g & 1) + (q4 >> 1)) & 3;
        const unsigned wbase = lds0 + (4 * (g >> 1) + (g & 1)) * 512 + q4 * 128 + (lane & 3) * 8;
        unsigned p0 = wbase + (fw << 5), p1 = wbase + ((1 ^ fw) << 5), p2 = wbase + ((2 ^ fw) << 5), p3 = wbase + ((3 ^ fw) << 5);
        const char* fop = ws + WS_FO + (2 * rt + (w3 >> 1)) * 32 * FS;
        unsigned fvo = r * FS;
        asm volatile("s_waitcnt vmcnt(0)" : "+{v[64:95]}"(rw0), "+{v[96:127]}"(rw1), "+v"(p0), "+v"(p1), "+v"(p2), "+v"(p3), "+v"(fvo) :: "memory");
        WAIT_LGKM0_BAR();
        f32x4 c[4]; u64 fl;
        lds_wait(lds + L_GO + w3 * 4);
#define ACC_OUT [c0] "=&{v[32:35]}"(c[0]), [c1] "=&{v[36:39]}"(c[1]), [c2] "=&{v[40:43]}"(c[2]), [c3] "=&{v[44:47]}"(c[3])
        asm volatile(PROJ_A : ACC_OUT, [fl] "=&v"(fl)
                     : [p0] "v"(p0), [p1] "v"(p1), [p2] "v"(p2), [p3] "v"(p3), [fo] "v"(fvo), [fp] "s"(fop), "{v[64:95]}"(rw0), "{v[96:127]}"(rw1) : "memory", CLOB_RING);
        const bool early = __all(fl == tag);
        if (early) LOAD_ROWS(ws + WS_OB); else fl = gld8_sc1(fop, fvo);
        f32x32 wq0, wq1, wq2, wq3;
        asm volatile(PROJ_B_W : "=&{v[128:159]}"(wq0), "=&{v[160:191]}"(wq1), "=&{v[192:223]}"(wq2), "=&{v[224:255]}"(wq3)
                     : [p0] "v"(p0 + L_WOB0), [p1] "v"(p1 + L_WOB0), [p2] "v"(p2 + L_WOB0), [p3] "v"(p3 + L_WOB0) : "memory");
        auto pack = [&](int j) -> u32x4 {
            const f32x4 x = c[2 * j], y = c[2 * j + 1];
            auto s0 = __builtin_amdgcn_permlane16_swap(cvtpk(x[0], x[1]), cvtpk(y[0], y[1]), false, false);
            auto s1 = __builtin_amdgcn_permlane16_swap(cvtpk(x[2], x[3]), cvtpk(y[2], y[3]), false, false);
            return u32x4{s0[0], s1[0], s0[1], s1[1]};
        };
        u32x4 ra0 = pack(0), ra1 = pack(1);
        const unsigned oc = cs * 128 + (g & 1) * 32 + (g >> 1) * 16;
        unsigned oa_ = (64 * rt + 32 * (w3 & 1) + 2 * tk + (w3 >> 1)) * 2048 + oc, ob_ = (64 * rt + 16 * w3 + tk) * 2048 + 1024 + oc;
        p0 += L_WOB1; p1 += L_WOB1; p2 += L_WOB1; p3 += L_WOB1;
        asm volatile("" : "+v"(ra0), "+v"(ra1), "+v"(oa_), "+v"(ob_), "+v"(p0), "+v"(p1), "+v"(p2), "+v"(p3));
        if (!early) {
            asm volatile("s_waitcnt vmcnt(0)" : "+v"(fl) :: "memory");
            if (!__all(fl == tag)) poll_flags(fop, fvo, tag);
            LOAD_ROWS(ws + WS_OB);
        }
        asm volatile(PROJ_B : ACC_OUT, "+{v[64:95]}"(rw0), "+{v[96:127]}"(rw1), "+{v[128:159]}"(wq0), "+{v[160:191]}"(wq1), "+{v[192:223]}"(wq2), "+{v[224:255]}"(wq3)
                     : [p0] "v"(p0), [p1] "v"(p1), [p2] "v"(p2), [p3] "v"(p3) : "memory");
        gst16_sc1(out, ob_, pack(0)); gst16_sc1(out, ob_ + 64, pack(1));
        gst16_sc1(out, oa_, ra0); gst16_sc1(out, oa_ + 64, ra1);
    }
}

extern "C" void kernel_launch(void* const* d_in, const int* in_sizes, int n_in, void* d_out, int out_size, void* d_ws, size_t ws_size,
                              hipStream_t stream) {
    (void)in_sizes; (void)n_in; (void)out_size; (void)ws_size;
    (void)hipFuncSetAttribute((const void*)fused_kernel, hipFuncAttributeMaxDynamicSharedMemorySize, LDS_BYTES);
    fused_kernel<<<256, 512, LDS_BYTES, stream>>>((const char*)d_in[0], (const char*)d_in[1], (const char*)d_in[2], (const char*)d_in[3],
                                                  (const char*)d_in[4], (const char*)d_in[5], (char*)d_out, (char*)d_ws);
}
```
